```python
import torch

def _softmax(x, axis=-1):
    return torch.softmax(x, dim=axis)
def _gelu(x):
    return torch.nn.functional.gelu(x, approximate="tanh")
def _silu(x):
    return torch.nn.functional.silu(x)
def _sigmoid(x):
    return torch.sigmoid(x)
def _layernorm(x, axis=-1, eps=1e-5):
    m = x.mean(dim=axis, keepdim=True)
    v = x.var(dim=axis, keepdim=True, unbiased=False)
    return (x - m) / torch.sqrt(v + eps)
def _scatter_add(tgt, idx, src, axis=0):
    return tgt.clone().index_add_(axis, idx, src)
def _segment_sum(src, seg, n):
    out = torch.zeros((n,) + src.shape[1:], dtype=src.dtype)
    return out.index_add_(0, seg, src)
def _pad(x, pw):
    flat = tuple(v for pair in reversed(pw) for v in pair)
    return torch.nn.functional.pad(x, flat)

kernel_name = 'recurrent_scan__sliding_block_cached__geglu_ffn__decode'

def setup_inputs():

    _w = (lambda a: (lambda t: t.to(torch.bfloat16) if t.is_floating_point() else t)(torch.as_tensor(a)))
    def _A_setup():
        B, Sq, D = 1, 1, 448
        Hq, Hkv, Sk, Dh = 16, 8, 32768, 256
        assert Hq % Hkv == 0, (Hq, Hkv)
        return {
            "x": _w((lambda *s: torch.randn(*s, dtype=torch.bfloat16) * 0.05)(B, Sq, D)),
            "k_cache": _w((lambda *s: torch.randn(*s, dtype=torch.bfloat16) * 1.6)(B, Hkv, Sk, Dh)),
            "v_cache": _w((lambda *s: torch.randn(*s, dtype=torch.bfloat16) * 0.2)(B, Hkv, Sk, Dh)),
            "wq": _w((lambda *s: torch.randn(*s, dtype=torch.bfloat16) * 1.6)(D, Hq * Dh) * (D ** -0.5)),
            "wo": _w((lambda *s: torch.randn(*s, dtype=torch.bfloat16) * 0.2)(Hq * Dh, D) * ((Hq * Dh) ** -0.5)),
        }
    _A_in = _A_setup()

    def _B_setup():
        B, S, D, F = 1, 1, 448, 1792
        return {
            "x": _w((lambda *s: torch.randn(*s, dtype=torch.bfloat16) * 0.05)(B, S, D)),
            "w_gate": _w((lambda *s: torch.randn(*s, dtype=torch.bfloat16) * 0.1)(D, F)),
            "w_up": _w((lambda *s: torch.randn(*s, dtype=torch.bfloat16) * 0.1)(D, F)),
            "w_down": _w((lambda *s: torch.randn(*s, dtype=torch.bfloat16) * 0.1)(F, D)),
        }
    _B_in = _B_setup()

    return {
        "A_x": _A_in["x"],
        "A_k_cache": _A_in["k_cache"],
        "A_v_cache": _A_in["v_cache"],
        "A_wq": _A_in["wq"],
        "A_wo": _A_in["wo"],
        "B_x": _B_in["x"],
        "B_w_gate": _B_in["w_gate"],
        "B_w_up": _B_in["w_up"],
        "B_w_down": _B_in["w_down"],
    }

def reference(A_x, A_k_cache, A_v_cache, A_wq, A_wo, B_x, B_w_gate, B_w_up, B_w_down):
    def _A_ref(x, k_cache, v_cache, wq, wo):
        B, Sq, D = x.shape
        Hq, Dh = 16, 256
        Hkv, Sk = k_cache.shape[1], k_cache.shape[2]
        W = min(4096, max(256, Sk // 4))
        rep = Hq // Hkv
        q = (lambda x, *d: x.permute(*d))((x @ wq).reshape(B, Sq, Hq, Dh), 0, 2, 1, 3)
        k = (lambda x, n, axis: torch.repeat_interleave(x, n, dim=axis))(k_cache[:, :, Sk - W:, :], rep, axis=1)
        v = (lambda x, n, axis: torch.repeat_interleave(x, n, dim=axis))(v_cache[:, :, Sk - W:, :], rep, axis=1)
        scores = torch.einsum("bhqd,bhkd->bhqk", q, k) * (Dh ** -0.5)
        p = _softmax(scores, axis=-1)
        o = torch.einsum("bhqk,bhkd->bhqd", p, v)
        return (lambda x, *d: x.permute(*d))(o, 0, 2, 1, 3).reshape(B, Sq, Hq * Dh) @ wo

    def _B_ref(x, w_gate, w_up, w_down):
        return (_gelu(x @ w_gate) * (x @ w_up)) @ w_down

    x = A_x
    xn = _layernorm(x, axis=-1)
    s = _A_ref(xn, A_k_cache, A_v_cache, A_wq, A_wo)
    g = _silu(_B_ref(xn, B_w_gate, B_w_up, B_w_down))
    return x + s * g
```

```cpp
#include <hip/hip_runtime.h>
#include <hip/hip_bf16.h>
#include <stdint.h>

typedef unsigned v4u __attribute__((ext_vector_type(4)));
constexpr int D = 448, HQ = 16, HKV = 8, SK = 32768, DH = 256, W = 4096, F = 1792;

__device__ __forceinline__ float bf2f(unsigned short b) { return __uint_as_float((unsigned)b << 16); }
__device__ __forceinline__ float rbf(float f) { return __bfloat162float(__float2bfloat16(f)); }

__device__ void layernorm(const unsigned short* x, float* xn, float* red) {
  int t = threadIdx.x;
  float s = 0.f;
  for (int i = t; i < D; i += blockDim.x) s += bf2f(x[i]);
  red[t] = s; __syncthreads();
  float tot = 0.f; for (int i = 0; i < (int)blockDim.x; ++i) tot += red[i];
  __syncthreads();
  float mean = tot / D;
  float v = 0.f;
  for (int i = t; i < D; i += blockDim.x) { float d = bf2f(x[i]) - mean; v += d * d; }
  red[t] = v; __syncthreads();
  float vt = 0.f; for (int i = 0; i < (int)blockDim.x; ++i) vt += red[i];
  __syncthreads();
  float mb = rbf(mean), vb = rbf(vt / D);
  float den = rbf(sqrtf(rbf(vb + 1e-5f)));
  for (int i = t; i < D; i += blockDim.x) xn[i] = rbf(rbf(bf2f(x[i]) - mb) / den);
  __syncthreads();
}

__global__ void __launch_bounds__(256) k1(const unsigned short* x, const unsigned short* wq, const unsigned short* wg, const unsigned short* wu, float* q, float* h) {
  __shared__ float xn[D]; __shared__ float red[256];
  layernorm(x, xn, red);
  int c = blockIdx.x * 256 + threadIdx.x;
  if (c < HQ * DH) {
    float a = 0.f;
    for (int i = 0; i < D; ++i) a += xn[i] * bf2f(wq[(size_t)i * HQ * DH + c]);
    q[c] = rbf(a);
  } else if (c < HQ * DH + F) {
    int f = c - HQ * DH;
    float a = 0.f, b = 0.f;
    for (int i = 0; i < D; ++i) { a += xn[i] * bf2f(wg[(size_t)i * F + f]); b += xn[i] * bf2f(wu[(size_t)i * F + f]); }
    a = rbf(a); b = rbf(b);
    float g = 0.5f * a * (1.f + tanhf(0.7978845608028654f * (a + 0.044715f * a * a * a)));
    h[f] = rbf(rbf(g) * b);
  }
}

__global__ void __launch_bounds__(256) k2(const unsigned short* K, const unsigned short* V, const float* q, float* po, float* pml) {
  __shared__ float qs[2 * DH]; __shared__ float sc[2][128]; __shared__ float ml[2][2];
  int kvh = blockIdx.x & 7, s = blockIdx.x >> 3, t = threadIdx.x;
  for (int i = t; i < 2 * DH; i += 256) qs[i] = q[kvh * 2 * DH + i];
  __syncthreads();
  const unsigned short* Kb = K + ((size_t)kvh * SK + (SK - W) + s * 128) * DH;
  const unsigned short* Vb = V + ((size_t)kvh * SK + (SK - W) + s * 128) * DH;
  { int key = t & 127, hh = t >> 7; float a = 0.f;
    for (int d = 0; d < DH; ++d) a += qs[hh * DH + d] * bf2f(Kb[(size_t)key * DH + d]);
    sc[hh][key] = rbf(a) * 0.0625f; }
  __syncthreads();
  if (t < 2) { float m = -INFINITY; for (int i = 0; i < 128; ++i) m = fmaxf(m, sc[t][i]);
    float l = 0.f; for (int i = 0; i < 128; ++i) { float p = __expf(sc[t][i] - m); sc[t][i] = p; l += p; }
    ml[t][0] = m; ml[t][1] = l; }
  __syncthreads();
  float a0 = 0.f, a1 = 0.f;
  for (int i = 0; i < 128; ++i) { float v = bf2f(Vb[(size_t)i * DH + t]); a0 += sc[0][i] * v; a1 += sc[1][i] * v; }
  po[((size_t)(kvh * 2 + 0) * 32 + s) * DH + t] = a0;
  po[((size_t)(kvh * 2 + 1) * 32 + s) * DH + t] = a1;
  if (t < 2) { pml[((kvh * 2 + t) * 32 + s) * 2] = ml[t][0]; pml[((kvh * 2 + t) * 32 + s) * 2 + 1] = ml[t][1]; }
}

__global__ void __launch_bounds__(256) k3(const float* po, const float* pml, float* o) {
  int h = blockIdx.x, t = threadIdx.x;
  float M = -INFINITY; for (int s = 0; s < 32; ++s) M = fmaxf(M, pml[(h * 32 + s) * 2]);
  float L = 0.f, a = 0.f;
  for (int s = 0; s < 32; ++s) { float w = __expf(pml[(h * 32 + s) * 2] - M); L += w * pml[(h * 32 + s) * 2 + 1]; a += w * po[((size_t)h * 32 + s) * DH + t]; }
  o[h * DH + t] = rbf(a / L);
}

__global__ void __launch_bounds__(256) k4(const unsigned short* x, const unsigned short* wo, const unsigned short* wd, const float* o, const float* h, __hip_bfloat16* out) {
  __shared__ float r1[256], r2[256];
  int j = blockIdx.x, t = threadIdx.x;
  float a = 0.f, b = 0.f;
  for (int i = t; i < HQ * DH; i += 256) a += o[i] * bf2f(wo[(size_t)i * D + j]);
  for (int i = t; i < F; i += 256) b += h[i] * bf2f(wd[(size_t)i * D + j]);
  r1[t] = a; r2[t] = b; __syncthreads();
  if (t == 0) { float s = 0.f, g = 0.f; for (int i = 0; i < 256; ++i) { s += r1[i]; g += r2[i]; }
    s = rbf(s); g = rbf(g); g = rbf(g / (1.f + __expf(-g)));
    out[j] = __float2bfloat16(bf2f(x[j]) + rbf(s * g)); }
}

extern "C" void kernel_launch(void* const* d_in, const int* in_sizes, int n_in, void* d_out, int out_size, void* d_ws, size_t ws_size, hipStream_t stream) {
  const unsigned short* x = (const unsigned short*)d_in[0];
  const unsigned short* K = (const unsigned short*)d_in[1];
  const unsigned short* V = (const unsigned short*)d_in[2];
  const unsigned short* wq = (const unsigned short*)d_in[3];
  const unsigned short* wo = (const unsigned short*)d_in[4];
  const unsigned short* wg = (const unsigned short*)d_in[6];
  const unsigned short* wu = (const unsigned short*)d_in[7];
  const unsigned short* wd = (const unsigned short*)d_in[8];
  float* ws = (float*)d_ws;
  float* q = ws; float* h = ws + 4096; float* o = ws + 8192; float* pml = ws + 16384; float* po = ws + 32768;
  k1<<<(HQ * DH + F) / 256, 256, 0, stream>>>(x, wq, wg, wu, q, h);
  k2<<<256, 256, 0, stream>>>(K, V, q, po, pml);
  k3<<<16, 256, 0, stream>>>(po, pml, o);
  k4<<<D, 256, 0, stream>>>(x, wo, wd, o, h, (__hip_bfloat16*)d_out);
}
```

```cpp
#include <hip/hip_runtime.h>

extern "C" __global__ void __launch_bounds__(640, 3) __attribute__((aligned(4096)))
fused_decode(const void* K, const void* x, const void* wq, void* ws, const void* V, const void* wg, const void* wu, const void* wo, const void* wd, void* out) {
  __shared__ __attribute__((aligned(16))) char smem[4096];
  asm volatile(
    "s_mov_b32 s16, s2\n"
    "s_load_dwordx2 s[2:3], s[0:1], 0x0\n"
    "s_load_dwordx4 s[4:7], s[0:1], 0x8\n"
    "s_load_dwordx8 s[8:15], s[0:1], 0x18\n"
    "v_readfirstlane_b32 s24, v0\n"
    "v_and_b32 v1, 63, v0\n"
    "s_and_b32 s26, s16, 7\n"
    "s_lshr_b32 s27, s16, 3\n"
    "s_mov_b32 s31, 0x20000\n"
    "s_lshr_b32 s24, s24, 6\n"
    "s_cbranch_scc0 .LA0_fused_decode\n"
    "s_lshl_b32 s28, s26, 8\n"
    "s_add_u32 s28, s28, s27\n"
    "s_add_u32 s28, s28, 0xe0\n"
    "s_lshl_b32 s33, s28, 16\n"
    "s_mov_b32 s30, 0x10000\n"
    "s_mov_b32 s53, 0xffff\n"
    "s_lshl_b32 s42, s27, 12\n"
    "s_and_b32 s42, s42, s53\n"
    "s_sub_u32 s25, s24, 2\n"
    "s_cmp_lt_u32 s25, 4\n"
    "s_cbranch_scc0 .LNK_fused_decode\n"
    "s_lshl_b32 s43, s25, 10\n"
    "v_lshl_add_u32 v8, v1, 4, s43\n"
    "s_movk_i32 s55, 4096\n"
    "s_setprio 2\n"
    "s_waitcnt lgkmcnt(0)\n"
    "s_add_u32 s28, s2, s33\n"
    "s_addc_u32 s29, s3, 0\n"
    "s_and_b32 s29, s29, s53\n"
    "s_barrier\n"
    "buffer_load_dwordx4 v[12:15], v8, s[28:31], s42 offen nt\n"
    "s_add_u32 s42, s42, s55\n"
    "s_and_b32 s42, s42, s53\n"
    "buffer_load_dwordx4 v[16:19], v8, s[28:31], s42 offen nt\n"
    "s_add_u32 s42, s42, s55\n"
    "s_and_b32 s42, s42, s53\n"
    "buffer_load_dwordx4 v[20:23], v8, s[28:31], s42 offen nt\n"
    "s_add_u32 s42, s42, s55\n"
    "s_and_b32 s42, s42, s53\n"
    "buffer_load_dwordx4 v[24:27], v8, s[28:31], s42 offen nt\n"
    "s_add_u32 s42, s42, s55\n"
    "s_and_b32 s42, s42, s53\n"
    "buffer_load_dwordx4 v[28:31], v8, s[28:31], s42 offen nt\n"
    "s_add_u32 s42, s42, s55\n"
    "s_and_b32 s42, s42, s53\n"
    "v_lshrrev_b32 v2, 3, v1\n"
    "v_and_b32 v3, 7, v1\n"
    "v_mov_b32 v4, 1\n"
    "v_mov_b32 v5, 0\n"
    "v_mov_b32 v6, 0xfffff000\n"
    "s_mov_b32 s39, s31\n"
    "s_mul_i32 s54, s26, 106496\n"
    "s_add_u32 s54, s54, 20480\n"
    "s_mul_i32 s35, s27, 3328\n"
    "s_add_u32 s35, s35, s54\n"
    "s_mov_b32 s38, 1789952\n"
    "s_mov_b32 s40, 0x05040100\n"
    "s_mov_b32 s41, 0x07060302\n"
    "s_barrier\n"
    "s_waitcnt vmcnt(4)\n"
    "buffer_load_dwordx4 v[32:35], v8, s[28:31], s42 offen nt\n"
    "s_add_u32 s42, s42, s55\n"
    "s_and_b32 s42, s42, s53\n"
    "s_waitcnt vmcnt(4)\n"
    "buffer_load_dwordx4 v[36:39], v8, s[28:31], s42 offen nt\n"
    "s_add_u32 s42, s42, s55\n"
    "s_and_b32 s42, s42, s53\n"
    "s_waitcnt vmcnt(4)\n"
    "buffer_load_dwordx4 v[40:43], v8, s[28:31], s42 offen nt\n"
    "s_add_u32 s42, s42, s55\n"
    "s_and_b32 s42, s42, s53\n"
    "s_waitcnt vmcnt(4)\n"
    "buffer_load_dwordx4 v[44:47], v8, s[28:31], s42 offen nt\n"
    "s_add_u32 s42, s42, s55\n"
    "s_and_b32 s42, s42, s53\n"
    "s_waitcnt vmcnt(4)\n"
    "buffer_load_dwordx4 v[48:51], v8, s[28:31], s42 offen nt\n"
    "s_add_u32 s42, s42, s55\n"
    "s_and_b32 s42, s42, s53\n"
    "s_waitcnt vmcnt(4)\n"
    "buffer_load_dwordx4 v[52:55], v8, s[28:31], s42 offen nt\n"
    "s_add_u32 s42, s42, s55\n"
    "s_and_b32 s42, s42, s53\n"
    "s_waitcnt vmcnt(4)\n"
    "buffer_load_dwordx4 v[56:59], v8, s[28:31], s42 offen nt\n"
    "s_add_u32 s42, s42, s55\n"
    "s_and_b32 s42, s42, s53\n"
    "s_waitcnt vmcnt(4)\n"
    "buffer_load_dwordx4 v[60:63], v8, s[28:31], s42 offen nt\n"
    "s_add_u32 s42, s42, s55\n"
    "s_and_b32 s42, s42, s53\n"
    "s_waitcnt vmcnt(4)\n"
    "buffer_load_dwordx4 v[64:67], v8, s[28:31], s42 offen nt\n"
    "s_add_u32 s42, s42, s55\n"
    "s_and_b32 s42, s42, s53\n"
    "s_waitcnt vmcnt(4)\n"
    "buffer_load_dwordx4 v[68:71], v8, s[28:31], s42 offen nt\n"
    "s_add_u32 s42, s42, s55\n"
    "s_and_b32 s42, s42, s53\n"
    "s_waitcnt vmcnt(4)\n"
    "buffer_load_dwordx4 v[72:75], v8, s[28:31], s42 offen nt\n"
    "s_mov_b64 exec, 1\n"
    "ds_add_u32 v5, v4 offset:3012\n"
    "s_mov_b64 exec, -1\n"
    "s_branch .LKB_fused_decode\n"
    ".LNK_fused_decode:\n"
    "s_barrier\n"
    "s_barrier\n"
    "s_movk_i32 s55, 4096\n"
    "v_lshrrev_b32 v2, 3, v1\n"
    "v_and_b32 v3, 7, v1\n"
    "v_mov_b32 v4, 1\n"
    "v_mov_b32 v5, 0\n"
    "v_mov_b32 v6, 0xfffff000\n"
    "s_mov_b32 s39, s31\n"
    "s_mul_i32 s54, s26, 106496\n"
    "s_add_u32 s54, s54, 20480\n"
    "s_mul_i32 s35, s27, 3328\n"
    "s_add_u32 s35, s35, s54\n"
    "s_mov_b32 s38, 1789952\n"
    "s_mov_b32 s40, 0x05040100\n"
    "s_mov_b32 s41, 0x07060302\n"
    "s_cmp_eq_u32 s24, 1\n"
    "s_cbranch_scc1 .LA1B_fused_decode\n"
    "s_branch .LV_fused_decode\n"
    ".LA0_fused_decode:\n"
    "s_lshr_b32 s33, s27, 3\n"
    "s_mul_i32 s42, s33, 917504\n"
    "s_and_b32 s43, s27, 7\n"
    "s_lshl_b32 s44, s26, 3\n"
    "s_add_u32 s43, s43, s44\n"
    "s_lshl_b32 s43, s43, 7\n"
    "s_add_u32 s33, s42, s43\n"
    "v_lshlrev_b32 v8, 4, v1\n"
    "v_lshrrev_b32 v9, 3, v1\n"
    "v_lshlrev_b32 v9, 14, v9\n"
    "v_and_b32 v10, 0x70, v8\n"
    "v_or_b32 v9, v9, v10\n"
    "s_mov_b32 s30, 3670016\n"
    "s_movk_i32 s46, 0x380\n"
    "s_mov_b32 s47, s31\n"
    "s_movk_i32 s55, 0x2000\n"
    "s_mov_b32 s49, 0x1e000\n"
    "s_setprio 3\n"
    "s_waitcnt lgkmcnt(0)\n"
    "s_mov_b32 s44, s4\n"
    "s_and_b32 s45, s5, 0xffff\n"
    "s_mov_b32 s28, s6\n"
    "s_and_b32 s29, s7, 0xffff\n"
    "buffer_load_dwordx4 v[68:71], v8, s[44:47], 0 offen\n"
    "buffer_load_dwordx4 v[12:15], v9, s[28:31], s33 offen nt\n"
    "s_add_u32 s33, s33, s55\n"
    "buffer_load_dwordx4 v[16:19], v9, s[28:31], s33 offen nt\n"
    "s_add_u32 s33, s33, s49\n"
    "buffer_load_dwordx4 v[20:23], v9, s[28:31], s33 offen nt\n"
    "s_add_u32 s33, s33, s55\n"
    "buffer_load_dwordx4 v[24:27], v9, s[28:31], s33 offen nt\n"
    "s_add_u32 s33, s33, s49\n"
    "buffer_load_dwordx4 v[28:31], v9, s[28:31], s33 offen nt\n"
    "s_add_u32 s33, s33, s55\n"
    "buffer_load_dwordx4 v[32:35], v9, s[28:31], s33 offen nt\n"
    "s_add_u32 s33, s33, s49\n"
    "buffer_load_dwordx4 v[36:39], v9, s[28:31], s33 offen nt\n"
    "s_add_u32 s33, s33, s55\n"
    "buffer_load_dwordx4 v[40:43], v9, s[28:31], s33 offen nt\n"
    "s_add_u32 s33, s33, s49\n"
    "buffer_load_dwordx4 v[44:47], v9, s[28:31], s33 offen nt\n"
    "s_add_u32 s33, s33, s55\n"
    "buffer_load_dwordx4 v[48:51], v9, s[28:31], s33 offen nt\n"
    "s_add_u32 s33, s33, s49\n"
    "buffer_load_dwordx4 v[52:55], v9, s[28:31], s33 offen nt\n"
    "s_add_u32 s33, s33, s55\n"
    "buffer_load_dwordx4 v[56:59], v9, s[28:31], s33 offen nt\n"
    "s_add_u32 s33, s33, s49\n"
    "buffer_load_dwordx4 v[60:63], v9, s[28:31], s33 offen nt\n"
    "s_add_u32 s33, s33, s55\n"
    "buffer_load_dwordx4 v[64:67], v9, s[28:31], s33 offen nt\n"
    "s_barrier\n"
    "v_lshrrev_b32 v2, 3, v1\n"
    "v_and_b32 v3, 7, v1\n"
    "v_mov_b32 v4, 1\n"
    "v_mov_b32 v5, 0\n"
    "v_mov_b32 v6, 0xfffff000\n"
    "s_mov_b32 s39, s31\n"
    "s_mul_i32 s54, s26, 106496\n"
    "s_add_u32 s54, s54, 20480\n"
    "s_mul_i32 s35, s27, 3328\n"
    "s_add_u32 s35, s35, s54\n"
    "s_mov_b32 s38, 1789952\n"
    "s_mov_b32 s40, 0x05040100\n"
    "s_mov_b32 s41, 0x07060302\n"
    "v_lshlrev_b32 v10, 2, v1\n"
    "ds_write_b32 v10, v5 offset:2816\n"
    "s_mov_b32 s36, s8\n"
    "s_and_b32 s37, s9, 0xffff\n"
    "s_waitcnt lgkmcnt(0)\n"
    "s_barrier\n"
    "buffer_load_dwordx4 v[100:103], v5, s[36:39], 0 offen sc1\n"
    "s_lshl_b32 s42, s26, 7\n"
    "s_add_u32 s42, s42, 1024\n"
    "s_cmp_eq_u32 s27, 0\n"
    "s_cselect_b32 s42, s42, 0x80000000\n"
    "v_mov_b32 v10, s42\n"
    "s_mov_b64 exec, 1\n"
    "buffer_store_dword v5, v10, s[36:39], 0 offen sc1\n"
    "s_mov_b64 exec, -1\n"
    "s_load_dwordx2 s[18:19], s[0:1], 0x38\n"
    "s_load_dwordx4 s[20:23], s[0:1], 0x40\n"
    "s_waitcnt vmcnt(16)\n"
    "v_lshlrev_b32 v72, 16, v68\n"
    "v_and_b32 v73, 0xffff0000, v68\n"
    "v_lshlrev_b32 v74, 16, v69\n"
    "v_and_b32 v75, 0xffff0000, v69\n"
    "v_lshlrev_b32 v76, 16, v70\n"
    "v_and_b32 v77, 0xffff0000, v70\n"
    "v_lshlrev_b32 v78, 16, v71\n"
    "v_and_b32 v79, 0xffff0000, v71\n"
    "v_add_f32 v80, v72, v73\n"
    "v_add_f32 v81, v74, v75\n"
    "v_add_f32 v82, v76, v77\n"
    "v_add_f32 v83, v78, v79\n"
    "v_add_f32 v80, v80, v81\n"
    "v_add_f32 v82, v82, v83\n"
    "v_add_f32 v80, v80, v82\n"
    "s_nop 1\n"
    "v_add_f32_dpp v80, v80, v80 quad_perm:[1,0,3,2] row_mask:0xf bank_mask:0xf bound_ctrl:1\n"
    "s_nop 1\n"
    "v_add_f32_dpp v80, v80, v80 quad_perm:[2,3,0,1] row_mask:0xf bank_mask:0xf bound_ctrl:1\n"
    "s_nop 1\n"
    "v_add_f32_dpp v80, v80, v80 row_half_mirror row_mask:0xf bank_mask:0xf bound_ctrl:1\n"
    "s_nop 1\n"
    "v_add_f32_dpp v80, v80, v80 row_ror:8 row_mask:0xf bank_mask:0xf bound_ctrl:1\n"
    "v_mov_b32 v81, v80\n"
    "s_nop 1\n"
    "v_permlane16_swap_b32 v80, v81\n"
    "v_add_f32 v80, v80, v81\n"
    "v_mov_b32 v81, v80\n"
    "s_nop 1\n"
    "v_permlane32_swap_b32 v80, v81\n"
    "v_add_f32 v80, v80, v81\n"
    "v_mul_f32 v80, 0x3b124925, v80\n"
    "v_cmp_gt_u32 vcc, 56, v1\n"
    "v_sub_f32 v82, v72, v80\n"
    "v_sub_f32 v83, v73, v80\n"
    "v_sub_f32 v84, v74, v80\n"
    "v_sub_f32 v85, v75, v80\n"
    "v_sub_f32 v86, v76, v80\n"
    "v_sub_f32 v87, v77, v80\n"
    "v_sub_f32 v88, v78, v80\n"
    "v_sub_f32 v89, v79, v80\n"
    "v_mul_f32 v90, v82, v82\n"
    "v_mul_f32 v91, v83, v83\n"
    "v_fmac_f32 v90, v84, v84\n"
    "v_fmac_f32 v91, v85, v85\n"
    "v_fmac_f32 v90, v86, v86\n"
    "v_fmac_f32 v91, v87, v87\n"
    "v_fmac_f32 v90, v88, v88\n"
    "v_fmac_f32 v91, v89, v89\n"
    "v_add_f32 v90, v90, v91\n"
    "v_cndmask_b32 v90, 0, v90, vcc\n"
    "s_nop 1\n"
    "v_add_f32_dpp v90, v90, v90 quad_perm:[1,0,3,2] row_mask:0xf bank_mask:0xf bound_ctrl:1\n"
    "s_nop 1\n"
    "v_add_f32_dpp v90, v90, v90 quad_perm:[2,3,0,1] row_mask:0xf bank_mask:0xf bound_ctrl:1\n"
    "s_nop 1\n"
    "v_add_f32_dpp v90, v90, v90 row_half_mirror row_mask:0xf bank_mask:0xf bound_ctrl:1\n"
    "s_nop 1\n"
    "v_add_f32_dpp v90, v90, v90 row_ror:8 row_mask:0xf bank_mask:0xf bound_ctrl:1\n"
    "v_mov_b32 v91, v90\n"
    "s_nop 1\n"
    "v_permlane16_swap_b32 v90, v91\n"
    "v_add_f32 v90, v90, v91\n"
    "v_mov_b32 v91, v90\n"
    "s_nop 1\n"
    "v_permlane32_swap_b32 v90, v91\n"
    "v_add_f32 v90, v90, v91\n"
    "v_mul_f32 v90, 0x3b124925, v90\n"
    "v_cvt_pk_bf16_f32 v80, v80, v90\n"
    "v_and_b32 v90, 0xffff0000, v80\n"
    "v_lshlrev_b32 v80, 16, v80\n"
    "v_add_f32 v90, 0x3727c5ac, v90\n"
    "v_cvt_pk_bf16_f32 v90, v90, v90\n"
    "v_lshlrev_b32 v90, 16, v90\n"
    "v_sqrt_f32 v90, v90\n"
    "v_sub_f32 v82, v72, v80\n"
    "v_sub_f32 v83, v73, v80\n"
    "v_sub_f32 v84, v74, v80\n"
    "v_sub_f32 v85, v75, v80\n"
    "v_sub_f32 v86, v76, v80\n"
    "v_sub_f32 v87, v77, v80\n"
    "v_sub_f32 v88, v78, v80\n"
    "v_sub_f32 v89, v79, v80\n"
    "v_cvt_pk_bf16_f32 v90, v90, v90\n"
    "v_lshlrev_b32 v90, 16, v90\n"
    "v_rcp_f32 v90, v90\n"
    "v_cvt_pk_bf16_f32 v72, v82, v83\n"
    "v_cvt_pk_bf16_f32 v73, v84, v85\n"
    "v_cvt_pk_bf16_f32 v74, v86, v87\n"
    "v_cvt_pk_bf16_f32 v75, v88, v89\n"
    "v_lshlrev_b32 v82, 16, v72\n"
    "v_and_b32 v83, 0xffff0000, v72\n"
    "v_lshlrev_b32 v84, 16, v73\n"
    "v_and_b32 v85, 0xffff0000, v73\n"
    "v_lshlrev_b32 v86, 16, v74\n"
    "v_and_b32 v87, 0xffff0000, v74\n"
    "v_lshlrev_b32 v88, 16, v75\n"
    "v_and_b32 v89, 0xffff0000, v75\n"
    "v_mul_f32 v82, v82, v90\n"
    "v_mul_f32 v83, v83, v90\n"
    "v_mul_f32 v84, v84, v90\n"
    "v_mul_f32 v85, v85, v90\n"
    "v_mul_f32 v86, v86, v90\n"
    "v_mul_f32 v87, v87, v90\n"
    "v_mul_f32 v88, v88, v90\n"
    "v_mul_f32 v89, v89, v90\n"
    "v_cvt_pk_bf16_f32 v72, v82, v83\n"
    "v_cvt_pk_bf16_f32 v73, v84, v85\n"
    "v_cvt_pk_bf16_f32 v74, v86, v87\n"
    "v_cvt_pk_bf16_f32 v75, v88, v89\n"
    "v_cndmask_b32 v72, 0, v72, vcc\n"
    "v_cndmask_b32 v73, 0, v73, vcc\n"
    "v_cndmask_b32 v74, 0, v74, vcc\n"
    "v_cndmask_b32 v75, 0, v75, vcc\n"
    "ds_write_b128 v8, v[72:75] offset:0\n"
    "ds_write_b128 v8, v[68:71] offset:3072\n"
    "s_lshr_b32 s33, s27, 3\n"
    "s_mul_i32 s42, s33, 224\n"
    "v_lshl_add_u32 v10, v2, 2, s42\n"
    "ds_read_b32 v76, v10 offset:0\n"
    "ds_read_b32 v77, v10 offset:32\n"
    "ds_read_b32 v78, v10 offset:64\n"
    "ds_read_b32 v79, v10 offset:96\n"
    "ds_read_b32 v80, v10 offset:128\n"
    "ds_read_b32 v81, v10 offset:160\n"
    "ds_read_b32 v82, v10 offset:192\n"
    "v_mov_b32 v84, 0\n"
    "v_mov_b32 v85, 0\n"
    "v_mov_b32 v86, 0\n"
    "v_mov_b32 v87, 0\n"
    "v_mov_b32 v88, 0\n"
    "v_mov_b32 v89, 0\n"
    "v_mov_b32 v90, 0\n"
    "v_mov_b32 v91, 0\n"
    "v_lshrrev_b32 v10, 1, v1\n"
    "v_and_b32 v10, 16, v10\n"
    "v_lshl_add_u32 v10, v3, 5, v10\n"
    "v_and_b32 v11, 0x18, v1\n"
    "v_cmp_eq_u32 vcc, 0, v11\n"
    "v_add_u32 v10, s35, v10\n"
    "v_bfrev_b32 v11, 1\n"
    "v_cndmask_b32 v10, v11, v10, vcc\n"
    "s_waitcnt lgkmcnt(0)\n"
    "s_waitcnt vmcnt(14)\n"
    "v_perm_b32 v92, v16, v12, s40\n"
    "v_perm_b32 v96, v16, v12, s41\n"
    "v_perm_b32 v93, v17, v13, s40\n"
    "v_perm_b32 v97, v17, v13, s41\n"
    "v_perm_b32 v94, v18, v14, s40\n"
    "v_perm_b32 v98, v18, v14, s41\n"
    "v_perm_b32 v95, v19, v15, s40\n"
    "v_perm_b32 v99, v19, v15, s41\n"
    "v_dot2c_f32_bf16 v84, v92, v76\n"
    "v_dot2c_f32_bf16 v85, v96, v76\n"
    "v_dot2c_f32_bf16 v86, v93, v76\n"
    "v_dot2c_f32_bf16 v87, v97, v76\n"
    "v_dot2c_f32_bf16 v88, v94, v76\n"
    "v_dot2c_f32_bf16 v89, v98, v76\n"
    "v_dot2c_f32_bf16 v90, v95, v76\n"
    "v_dot2c_f32_bf16 v91, v99, v76\n"
    "s_waitcnt vmcnt(12)\n"
    "v_perm_b32 v92, v24, v20, s40\n"
    "v_perm_b32 v96, v24, v20, s41\n"
    "v_perm_b32 v93, v25, v21, s40\n"
    "v_perm_b32 v97, v25, v21, s41\n"
    "v_perm_b32 v94, v26, v22, s40\n"
    "v_perm_b32 v98, v26, v22, s41\n"
    "v_perm_b32 v95, v27, v23, s40\n"
    "v_perm_b32 v99, v27, v23, s41\n"
    "v_dot2c_f32_bf16 v84, v92, v77\n"
    "v_dot2c_f32_bf16 v85, v96, v77\n"
    "v_dot2c_f32_bf16 v86, v93, v77\n"
    "v_dot2c_f32_bf16 v87, v97, v77\n"
    "v_dot2c_f32_bf16 v88, v94, v77\n"
    "v_dot2c_f32_bf16 v89, v98, v77\n"
    "v_dot2c_f32_bf16 v90, v95, v77\n"
    "v_dot2c_f32_bf16 v91, v99, v77\n"
    "s_waitcnt vmcnt(10)\n"
    "v_perm_b32 v92, v32, v28, s40\n"
    "v_perm_b32 v96, v32, v28, s41\n"
    "v_perm_b32 v93, v33, v29, s40\n"
    "v_perm_b32 v97, v33, v29, s41\n"
    "v_perm_b32 v94, v34, v30, s40\n"
    "v_perm_b32 v98, v34, v30, s41\n"
    "v_perm_b32 v95, v35, v31, s40\n"
    "v_perm_b32 v99, v35, v31, s41\n"
    "v_dot2c_f32_bf16 v84, v92, v78\n"
    "v_dot2c_f32_bf16 v85, v96, v78\n"
    "v_dot2c_f32_bf16 v86, v93, v78\n"
    "v_dot2c_f32_bf16 v87, v97, v78\n"
    "v_dot2c_f32_bf16 v88, v94, v78\n"
    "v_dot2c_f32_bf16 v89, v98, v78\n"
    "v_dot2c_f32_bf16 v90, v95, v78\n"
    "v_dot2c_f32_bf16 v91, v99, v78\n"
    "s_waitcnt vmcnt(8)\n"
    "v_perm_b32 v92, v40, v36, s40\n"
    "v_perm_b32 v96, v40, v36, s41\n"
    "v_perm_b32 v93, v41, v37, s40\n"
    "v_perm_b32 v97, v41, v37, s41\n"
    "v_perm_b32 v94, v42, v38, s40\n"
    "v_perm_b32 v98, v42, v38, s41\n"
    "v_perm_b32 v95, v43, v39, s40\n"
    "v_perm_b32 v99, v43, v39, s41\n"
    "v_dot2c_f32_bf16 v84, v92, v79\n"
    "v_dot2c_f32_bf16 v85, v96, v79\n"
    "v_dot2c_f32_bf16 v86, v93, v79\n"
    "v_dot2c_f32_bf16 v87, v97, v79\n"
    "v_dot2c_f32_bf16 v88, v94, v79\n"
    "v_dot2c_f32_bf16 v89, v98, v79\n"
    "v_dot2c_f32_bf16 v90, v95, v79\n"
    "v_dot2c_f32_bf16 v91, v99, v79\n"
    "s_waitcnt vmcnt(6)\n"
    "v_perm_b32 v92, v48, v44, s40\n"
    "v_perm_b32 v96, v48, v44, s41\n"
    "v_perm_b32 v93, v49, v45, s40\n"
    "v_perm_b32 v97, v49, v45, s41\n"
    "v_perm_b32 v94, v50, v46, s40\n"
    "v_perm_b32 v98, v50, v46, s41\n"
    "v_perm_b32 v95, v51, v47, s40\n"
    "v_perm_b32 v99, v51, v47, s41\n"
    "v_dot2c_f32_bf16 v84, v92, v80\n"
    "v_dot2c_f32_bf16 v85, v96, v80\n"
    "v_dot2c_f32_bf16 v86, v93, v80\n"
    "v_dot2c_f32_bf16 v87, v97, v80\n"
    "v_dot2c_f32_bf16 v88, v94, v80\n"
    "v_dot2c_f32_bf16 v89, v98, v80\n"
    "v_dot2c_f32_bf16 v90, v95, v80\n"
    "v_dot2c_f32_bf16 v91, v99, v80\n"
    "s_waitcnt vmcnt(4)\n"
    "v_perm_b32 v92, v56, v52, s40\n"
    "v_perm_b32 v96, v56, v52, s41\n"
    "v_perm_b32 v93, v57, v53, s40\n"
    "v_perm_b32 v97, v57, v53, s41\n"
    "v_perm_b32 v94, v58, v54, s40\n"
    "v_perm_b32 v98, v58, v54, s41\n"
    "v_perm_b32 v95, v59, v55, s40\n"
    "v_perm_b32 v99, v59, v55, s41\n"
    "v_dot2c_f32_bf16 v84, v92, v81\n"
    "v_dot2c_f32_bf16 v85, v96, v81\n"
    "v_dot2c_f32_bf16 v86, v93, v81\n"
    "v_dot2c_f32_bf16 v87, v97, v81\n"
    "v_dot2c_f32_bf16 v88, v94, v81\n"
    "v_dot2c_f32_bf16 v89, v98, v81\n"
    "v_dot2c_f32_bf16 v90, v95, v81\n"
    "v_dot2c_f32_bf16 v91, v99, v81\n"
    "s_waitcnt vmcnt(2)\n"
    "v_perm_b32 v92, v64, v60, s40\n"
    "v_perm_b32 v96, v64, v60, s41\n"
    "v_perm_b32 v93, v65, v61, s40\n"
    "v_perm_b32 v97, v65, v61, s41\n"
    "v_perm_b32 v94, v66, v62, s40\n"
    "v_perm_b32 v98, v66, v62, s41\n"
    "v_perm_b32 v95, v67, v63, s40\n"
    "v_perm_b32 v99, v67, v63, s41\n"
    "v_dot2c_f32_bf16 v84, v92, v82\n"
    "v_dot2c_f32_bf16 v85, v96, v82\n"
    "v_dot2c_f32_bf16 v86, v93, v82\n"
    "v_dot2c_f32_bf16 v87, v97, v82\n"
    "v_dot2c_f32_bf16 v88, v94, v82\n"
    "v_dot2c_f32_bf16 v89, v98, v82\n"
    "v_dot2c_f32_bf16 v90, v95, v82\n"
    "v_dot2c_f32_bf16 v91, v99, v82\n"
    "v_permlane32_swap_b32 v84, v88\n"
    "v_permlane32_swap_b32 v85, v89\n"
    "v_permlane32_swap_b32 v86, v90\n"
    "v_permlane32_swap_b32 v87, v91\n"
    "v_add_f32 v84, v84, v88\n"
    "v_add_f32 v85, v85, v89\n"
    "v_add_f32 v86, v86, v90\n"
    "v_add_f32 v87, v87, v91\n"
    "v_mov_b32 v88, v84\n"
    "v_mov_b32 v89, v85\n"
    "v_mov_b32 v90, v86\n"
    "v_mov_b32 v91, v87\n"
    "v_permlane16_swap_b32 v84, v88\n"
    "v_permlane16_swap_b32 v85, v89\n"
    "v_permlane16_swap_b32 v86, v90\n"
    "v_permlane16_swap_b32 v87, v91\n"
    "v_add_f32 v84, v84, v88\n"
    "v_add_f32 v85, v85, v89\n"
    "v_add_f32 v86, v86, v90\n"
    "v_add_f32 v87, v87, v91\n"
    "v_add_f32_dpp v84, v84, v84 row_ror:8 row_mask:0xf bank_mask:0xf bound_ctrl:1\n"
    "v_add_f32_dpp v85, v85, v85 row_ror:8 row_mask:0xf bank_mask:0xf bound_ctrl:1\n"
    "v_add_f32_dpp v86, v86, v86 row_ror:8 row_mask:0xf bank_mask:0xf bound_ctrl:1\n"
    "v_add_f32_dpp v87, v87, v87 row_ror:8 row_mask:0xf bank_mask:0xf bound_ctrl:1\n"
    "s_waitcnt vmcnt(0)\n"
    "v_add_u32 v100, 1, v100\n"
    "v_add_u32 v101, 1, v101\n"
    "v_add_u32 v102, 1, v102\n"
    "v_add_u32 v103, 1, v103\n"
    "v_and_b32 v100, 0xfff, v100\n"
    "v_and_b32 v101, 0xfff, v101\n"
    "v_and_b32 v102, 0xfff, v102\n"
    "v_and_b32 v103, 0xfff, v103\n"
    "v_readfirstlane_b32 s56, v100\n"
    "v_readfirstlane_b32 s57, v101\n"
    "v_readfirstlane_b32 s58, v102\n"
    "v_readfirstlane_b32 s59, v103\n"
    "ds_write_b128 v5, v[100:103] offset:2624\n"
    "v_add_u32 v88, 0x800, v84\n"
    "v_add_u32 v89, 0x800, v85\n"
    "v_add_u32 v90, 0x800, v86\n"
    "v_add_u32 v91, 0x800, v87\n"
    "v_and_or_b32 v88, v88, v6, s56\n"
    "v_and_or_b32 v89, v89, v6, s57\n"
    "v_and_or_b32 v90, v90, v6, s58\n"
    "v_and_or_b32 v91, v91, v6, s59\n"
    "buffer_store_dwordx4 v[88:91], v10, s[36:39], 0 offen offset:0 sc1\n"
    "s_mov_b64 exec, 1\n"
    "ds_add_u32 v5, v4 offset:3060\n"
    "s_mov_b64 exec, -1\n"
    "s_memrealtime s[46:47]\n"
    "v_lshrrev_b32 v60, 4, v1\n"
    "v_mul_u32_u24 v60, 3328, v60\n"
    "v_and_b32 v9, 15, v1\n"
    "v_lshl_add_u32 v60, v9, 4, v60\n"
    "v_add_u32 v60, s54, v60\n"
    "s_lshl_b32 s42, s26, 11\n"
    "s_add_u32 s43, s42, 4096\n"
    "v_lshl_add_u32 v98, v1, 4, s43\n"
    "s_lshl_b32 s42, s26, 7\n"
    "s_add_u32 s43, s42, 1024\n"
    "v_mov_b32 v99, s43\n"
    "v_add_u32 v61, 13312, v60\n"
    "v_add_u32 v62, 26624, v60\n"
    "v_add_u32 v63, 39936, v60\n"
    "v_add_u32 v64, 53248, v60\n"
    "v_add_u32 v65, 66560, v60\n"
    "v_add_u32 v66, 79872, v60\n"
    "v_add_u32 v67, 93184, v60\n"
    "s_waitcnt lgkmcnt(0)\n"
    "s_add_u32 s46, s46, 100\n"
    ".LT1_fused_decode:\n"
    "s_memrealtime s[48:49]\n"
    "s_waitcnt lgkmcnt(0)\n"
    "s_sub_u32 s43, s48, s46\n"
    "s_bitcmp1_b32 s43, 19\n"
    "s_cbranch_scc1 .LT1_fused_decode\n"
    "buffer_load_dwordx4 v[44:47], v98, s[36:39], 0 offen offset:0 sc1\n"
    "buffer_load_dwordx4 v[48:51], v98, s[36:39], 0 offen offset:1024 sc1\n"
    "s_mov_b32 s34, 131072\n"
    ".LG2_fused_decode:\n"
    "buffer_load_dwordx4 v[12:15], v60, s[36:39], 0 offen offset:0 sc1\n"
    "buffer_load_dwordx4 v[16:19], v61, s[36:39], 0 offen offset:0 sc1\n"
    "buffer_load_dwordx4 v[20:23], v62, s[36:39], 0 offen offset:0 sc1\n"
    "buffer_load_dwordx4 v[24:27], v63, s[36:39], 0 offen offset:0 sc1\n"
    "buffer_load_dwordx4 v[28:31], v64, s[36:39], 0 offen offset:0 sc1\n"
    "buffer_load_dwordx4 v[32:35], v65, s[36:39], 0 offen offset:0 sc1\n"
    "buffer_load_dwordx4 v[36:39], v66, s[36:39], 0 offen offset:0 sc1\n"
    "buffer_load_dwordx4 v[40:43], v67, s[36:39], 0 offen offset:0 sc1\n"
    "s_waitcnt vmcnt(7)\n"
    "v_xor_b32 v12, s56, v12\n"
    "v_xor_b32 v13, s57, v13\n"
    "v_xor_b32 v14, s58, v14\n"
    "v_xor_b32 v15, s59, v15\n"
    "v_or3_b32 v7, v12, v13, v14\n"
    "v_or_b32 v7, v7, v15\n"
    "s_waitcnt vmcnt(6)\n"
    "v_xor_b32 v16, s56, v16\n"
    "v_xor_b32 v17, s57, v17\n"
    "v_xor_b32 v18, s58, v18\n"
    "v_xor_b32 v19, s59, v19\n"
    "v_or3_b32 v7, v7, v16, v17\n"
    "v_or3_b32 v7, v7, v18, v19\n"
    "s_waitcnt vmcnt(5)\n"
    "v_xor_b32 v20, s56, v20\n"
    "v_xor_b32 v21, s57, v21\n"
    "v_xor_b32 v22, s58, v22\n"
    "v_xor_b32 v23, s59, v23\n"
    "v_or3_b32 v7, v7, v20, v21\n"
    "v_or3_b32 v7, v7, v22, v23\n"
    "s_waitcnt vmcnt(4)\n"
    "v_xor_b32 v24, s56, v24\n"
    "v_xor_b32 v25, s57, v25\n"
    "v_xor_b32 v26, s58, v26\n"
    "v_xor_b32 v27, s59, v27\n"
    "v_or3_b32 v7, v7, v24, v25\n"
    "v_or3_b32 v7, v7, v26, v27\n"
    "s_waitcnt vmcnt(3)\n"
    "v_xor_b32 v28, s56, v28\n"
    "v_xor_b32 v29, s57, v29\n"
    "v_xor_b32 v30, s58, v30\n"
    "v_xor_b32 v31, s59, v31\n"
    "v_or3_b32 v7, v7, v28, v29\n"
    "v_or3_b32 v7, v7, v30, v31\n"
    "s_waitcnt vmcnt(2)\n"
    "v_xor_b32 v32, s56, v32\n"
    "v_xor_b32 v33, s57, v33\n"
    "v_xor_b32 v34, s58, v34\n"
    "v_xor_b32 v35, s59, v35\n"
    "v_or3_b32 v7, v7, v32, v33\n"
    "v_or3_b32 v7, v7, v34, v35\n"
    "s_waitcnt vmcnt(1)\n"
    "v_xor_b32 v36, s56, v36\n"
    "v_xor_b32 v37, s57, v37\n"
    "v_xor_b32 v38, s58, v38\n"
    "v_xor_b32 v39, s59, v39\n"
    "v_or3_b32 v7, v7, v36, v37\n"
    "v_or3_b32 v7, v7, v38, v39\n"
    "s_waitcnt vmcnt(0)\n"
    "v_xor_b32 v40, s56, v40\n"
    "v_xor_b32 v41, s57, v41\n"
    "v_xor_b32 v42, s58, v42\n"
    "v_xor_b32 v43, s59, v43\n"
    "v_or3_b32 v7, v7, v40, v41\n"
    "v_or3_b32 v7, v7, v42, v43\n"
    "v_and_b32 v7, 0xfff, v7\n"
    "v_cmp_ne_u32 vcc, 0, v7\n"
    "s_nop 0\n"
    "s_cbranch_vccz .LG2_fused_decode_d\n"
    "s_mov_b64 exec, vcc\n"
    "s_sleep 8\n"
    "s_sub_u32 s34, s34, 1\n"
    "s_cbranch_scc0 .LG2_fused_decode\n"
    ".LG2_fused_decode_d:\n"
    "s_mov_b64 exec, -1\n"
    "v_add_f32 v12, v12, v20\n"
    "v_add_f32 v28, v28, v36\n"
    "v_add_f32 v13, v13, v21\n"
    "v_add_f32 v29, v29, v37\n"
    "v_add_f32 v14, v14, v22\n"
    "v_add_f32 v30, v30, v38\n"
    "v_add_f32 v15, v15, v23\n"
    "v_add_f32 v31, v31, v39\n"
    "v_add_f32 v12, v12, v28\n"
    "v_add_f32 v13, v13, v29\n"
    "v_add_f32 v14, v14, v30\n"
    "v_add_f32 v15, v15, v31\n"
    "v_add_f32 v16, v16, v24\n"
    "v_add_f32 v32, v32, v40\n"
    "v_add_f32 v17, v17, v25\n"
    "v_add_f32 v33, v33, v41\n"
    "v_add_f32 v18, v18, v26\n"
    "v_add_f32 v34, v34, v42\n"
    "v_add_f32 v19, v19, v27\n"
    "v_add_f32 v35, v35, v43\n"
    "v_add_f32 v16, v16, v32\n"
    "v_add_f32 v17, v17, v33\n"
    "v_add_f32 v18, v18, v34\n"
    "v_add_f32 v19, v19, v35\n"
    "v_cvt_pk_bf16_f32 v12, v12, v13\n"
    "v_cvt_pk_bf16_f32 v13, v14, v15\n"
    "v_cvt_pk_bf16_f32 v14, v16, v17\n"
    "v_cvt_pk_bf16_f32 v15, v18, v19\n"
    "v_lshlrev_b32 v16, 3, v1\n"
    "ds_write_b64 v16, v[12:13] offset:1024\n"
    "ds_write_b64 v16, v[14:15] offset:1536\n"
    "s_mov_b64 exec, 1\n"
    "ds_add_u32 v5, v4 offset:3008\n"
    "s_mov_b64 exec, -1\n"
    "s_branch .LA0T_fused_decode\n"
    ".p2align 12\n"
    ".LVBs_fused_decode:\n"
    "s_setpc_b64 s[48:49]\n"
    ".LVB_fused_decode:\n"
    ".LW18_fused_decode:\n"
    "ds_read_b32 v7, v5 offset:3024\n"
    "s_waitcnt lgkmcnt(0)\n"
    "v_cmp_gt_u32 vcc, 4, v7\n"
    "s_nop 0\n"
    "s_cbranch_vccz .LW18_fused_decode_d\n"
    "s_sleep 1\n"
    "s_branch .LW18_fused_decode\n"
    ".LW18_fused_decode_d:\n"
    "s_setprio 3\n"
    "ds_read_b128 v[76:79], v10 offset:2048\n"
    "ds_read_b128 v[80:83], v10 offset:2064\n"
    "ds_read_b128 v[84:87], v10 offset:2080\n"
    "ds_read_b128 v[88:91], v10 offset:2096\n"
    "ds_read2_b32 v[108:109], v11 offset0:0 offset1:2\n"
    "ds_read2_b32 v[110:111], v11 offset0:4 offset1:6\n"
    "ds_read2_b32 v[112:113], v11 offset0:8 offset1:10\n"
    "ds_read2_b32 v[114:115], v11 offset0:12 offset1:14\n"
    "ds_read_b128 v[116:119], v5 offset:2624\n"
    "s_waitcnt lgkmcnt(0)\n"
    "v_readfirstlane_b32 s56, v116\n"
    "v_readfirstlane_b32 s57, v117\n"
    "v_readfirstlane_b32 s58, v118\n"
    "v_readfirstlane_b32 s59, v119\n"
    "v_max3_f32 v116, v108, v109, v110\n"
    "v_max_f32 v116, v116, v111\n"
    "v_add_f32 v112, v112, v113\n"
    "v_add_f32 v114, v114, v115\n"
    "v_add_f32 v117, v112, v114\n"
    "v_perm_b32 v108, v16, v12, s40\n"
    "v_perm_b32 v112, v16, v12, s41\n"
    "v_perm_b32 v109, v17, v13, s40\n"
    "v_perm_b32 v113, v17, v13, s41\n"
    "v_perm_b32 v110, v18, v14, s40\n"
    "v_perm_b32 v114, v18, v14, s41\n"
    "v_perm_b32 v111, v19, v15, s40\n"
    "v_perm_b32 v115, v19, v15, s41\n"
    "v_dot2c_f32_bf16 v92, v108, v76\n"
    "v_dot2c_f32_bf16 v93, v112, v76\n"
    "v_dot2c_f32_bf16 v100, v108, v84\n"
    "v_dot2c_f32_bf16 v101, v112, v84\n"
    "v_dot2c_f32_bf16 v94, v109, v76\n"
    "v_dot2c_f32_bf16 v95, v113, v76\n"
    "v_dot2c_f32_bf16 v102, v109, v84\n"
    "v_dot2c_f32_bf16 v103, v113, v84\n"
    "v_dot2c_f32_bf16 v96, v110, v76\n"
    "v_dot2c_f32_bf16 v97, v114, v76\n"
    "v_dot2c_f32_bf16 v104, v110, v84\n"
    "v_dot2c_f32_bf16 v105, v114, v84\n"
    "v_dot2c_f32_bf16 v98, v111, v76\n"
    "v_dot2c_f32_bf16 v99, v115, v76\n"
    "v_dot2c_f32_bf16 v106, v111, v84\n"
    "v_dot2c_f32_bf16 v107, v115, v84\n"
    "v_perm_b32 v108, v24, v20, s40\n"
    "v_perm_b32 v112, v24, v20, s41\n"
    "v_perm_b32 v109, v25, v21, s40\n"
    "v_perm_b32 v113, v25, v21, s41\n"
    "v_perm_b32 v110, v26, v22, s40\n"
    "v_perm_b32 v114, v26, v22, s41\n"
    "v_perm_b32 v111, v27, v23, s40\n"
    "v_perm_b32 v115, v27, v23, s41\n"
    "v_dot2c_f32_bf16 v92, v108, v77\n"
    "v_dot2c_f32_bf16 v93, v112, v77\n"
    "v_dot2c_f32_bf16 v100, v108, v85\n"
    "v_dot2c_f32_bf16 v101, v112, v85\n"
    "v_dot2c_f32_bf16 v94, v109, v77\n"
    "v_dot2c_f32_bf16 v95, v113, v77\n"
    "v_dot2c_f32_bf16 v102, v109, v85\n"
    "v_dot2c_f32_bf16 v103, v113, v85\n"
    "v_dot2c_f32_bf16 v96, v110, v77\n"
    "v_dot2c_f32_bf16 v97, v114, v77\n"
    "v_dot2c_f32_bf16 v104, v110, v85\n"
    "v_dot2c_f32_bf16 v105, v114, v85\n"
    "v_dot2c_f32_bf16 v98, v111, v77\n"
    "v_dot2c_f32_bf16 v99, v115, v77\n"
    "v_dot2c_f32_bf16 v106, v111, v85\n"
    "v_dot2c_f32_bf16 v107, v115, v85\n"
    "v_perm_b32 v108, v32, v28, s40\n"
    "v_perm_b32 v112, v32, v28, s41\n"
    "v_perm_b32 v109, v33, v29, s40\n"
    "v_perm_b32 v113, v33, v29, s41\n"
    "v_perm_b32 v110, v34, v30, s40\n"
    "v_perm_b32 v114, v34, v30, s41\n"
    "v_perm_b32 v111, v35, v31, s40\n"
    "v_perm_b32 v115, v35, v31, s41\n"
    "v_dot2c_f32_bf16 v92, v108, v78\n"
    "v_dot2c_f32_bf16 v93, v112, v78\n"
    "v_dot2c_f32_bf16 v100, v108, v86\n"
    "v_dot2c_f32_bf16 v101, v112, v86\n"
    "v_dot2c_f32_bf16 v94, v109, v78\n"
    "v_dot2c_f32_bf16 v95, v113, v78\n"
    "v_dot2c_f32_bf16 v102, v109, v86\n"
    "v_dot2c_f32_bf16 v103, v113, v86\n"
    "v_dot2c_f32_bf16 v96, v110, v78\n"
    "v_dot2c_f32_bf16 v97, v114, v78\n"
    "v_dot2c_f32_bf16 v104, v110, v86\n"
    "v_dot2c_f32_bf16 v105, v114, v86\n"
    "v_dot2c_f32_bf16 v98, v111, v78\n"
    "v_dot2c_f32_bf16 v99, v115, v78\n"
    "v_dot2c_f32_bf16 v106, v111, v86\n"
    "v_dot2c_f32_bf16 v107, v115, v86\n"
    "v_perm_b32 v108, v40, v36, s40\n"
    "v_perm_b32 v112, v40, v36, s41\n"
    "v_perm_b32 v109, v41, v37, s40\n"
    "v_perm_b32 v113, v41, v37, s41\n"
    "v_perm_b32 v110, v42, v38, s40\n"
    "v_perm_b32 v114, v42, v38, s41\n"
    "v_perm_b32 v111, v43, v39, s40\n"
    "v_perm_b32 v115, v43, v39, s41\n"
    "v_dot2c_f32_bf16 v92, v108, v79\n"
    "v_dot2c_f32_bf16 v93, v112, v79\n"
    "v_dot2c_f32_bf16 v100, v108, v87\n"
    "v_dot2c_f32_bf16 v101, v112, v87\n"
    "v_dot2c_f32_bf16 v94, v109, v79\n"
    "v_dot2c_f32_bf16 v95, v113, v79\n"
    "v_dot2c_f32_bf16 v102, v109, v87\n"
    "v_dot2c_f32_bf16 v103, v113, v87\n"
    "v_dot2c_f32_bf16 v96, v110, v79\n"
    "v_dot2c_f32_bf16 v97, v114, v79\n"
    "v_dot2c_f32_bf16 v104, v110, v87\n"
    "v_dot2c_f32_bf16 v105, v114, v87\n"
    "v_dot2c_f32_bf16 v98, v111, v79\n"
    "v_dot2c_f32_bf16 v99, v115, v79\n"
    "v_dot2c_f32_bf16 v106, v111, v87\n"
    "v_dot2c_f32_bf16 v107, v115, v87\n"
    "s_waitcnt vmcnt(6)\n"
    "v_perm_b32 v108, v48, v44, s40\n"
    "v_perm_b32 v112, v48, v44, s41\n"
    "v_perm_b32 v109, v49, v45, s40\n"
    "v_perm_b32 v113, v49, v45, s41\n"
    "v_perm_b32 v110, v50, v46, s40\n"
    "v_perm_b32 v114, v50, v46, s41\n"
    "v_perm_b32 v111, v51, v47, s40\n"
    "v_perm_b32 v115, v51, v47, s41\n"
    "v_dot2c_f32_bf16 v92, v108, v80\n"
    "v_dot2c_f32_bf16 v93, v112, v80\n"
    "v_dot2c_f32_bf16 v100, v108, v88\n"
    "v_dot2c_f32_bf16 v101, v112, v88\n"
    "v_dot2c_f32_bf16 v94, v109, v80\n"
    "v_dot2c_f32_bf16 v95, v113, v80\n"
    "v_dot2c_f32_bf16 v102, v109, v88\n"
    "v_dot2c_f32_bf16 v103, v113, v88\n"
    "v_dot2c_f32_bf16 v96, v110, v80\n"
    "v_dot2c_f32_bf16 v97, v114, v80\n"
    "v_dot2c_f32_bf16 v104, v110, v88\n"
    "v_dot2c_f32_bf16 v105, v114, v88\n"
    "v_dot2c_f32_bf16 v98, v111, v80\n"
    "v_dot2c_f32_bf16 v99, v115, v80\n"
    "v_dot2c_f32_bf16 v106, v111, v88\n"
    "v_dot2c_f32_bf16 v107, v115, v88\n"
    "s_waitcnt vmcnt(4)\n"
    "v_perm_b32 v108, v56, v52, s40\n"
    "v_perm_b32 v112, v56, v52, s41\n"
    "v_perm_b32 v109, v57, v53, s40\n"
    "v_perm_b32 v113, v57, v53, s41\n"
    "v_perm_b32 v110, v58, v54, s40\n"
    "v_perm_b32 v114, v58, v54, s41\n"
    "v_perm_b32 v111, v59, v55, s40\n"
    "v_perm_b32 v115, v59, v55, s41\n"
    "v_dot2c_f32_bf16 v92, v108, v81\n"
    "v_dot2c_f32_bf16 v93, v112, v81\n"
    "v_dot2c_f32_bf16 v100, v108, v89\n"
    "v_dot2c_f32_bf16 v101, v112, v89\n"
    "v_dot2c_f32_bf16 v94, v109, v81\n"
    "v_dot2c_f32_bf16 v95, v113, v81\n"
    "v_dot2c_f32_bf16 v102, v109, v89\n"
    "v_dot2c_f32_bf16 v103, v113, v89\n"
    "v_dot2c_f32_bf16 v96, v110, v81\n"
    "v_dot2c_f32_bf16 v97, v114, v81\n"
    "v_dot2c_f32_bf16 v104, v110, v89\n"
    "v_dot2c_f32_bf16 v105, v114, v89\n"
    "v_dot2c_f32_bf16 v98, v111, v81\n"
    "v_dot2c_f32_bf16 v99, v115, v81\n"
    "v_dot2c_f32_bf16 v106, v111, v89\n"
    "v_dot2c_f32_bf16 v107, v115, v89\n"
    "s_waitcnt vmcnt(2)\n"
    "v_perm_b32 v108, v64, v60, s40\n"
    "v_perm_b32 v112, v64, v60, s41\n"
    "v_perm_b32 v109, v65, v61, s40\n"
    "v_perm_b32 v113, v65, v61, s41\n"
    "v_perm_b32 v110, v66, v62, s40\n"
    "v_perm_b32 v114, v66, v62, s41\n"
    "v_perm_b32 v111, v67, v63, s40\n"
    "v_perm_b32 v115, v67, v63, s41\n"
    "v_dot2c_f32_bf16 v92, v108, v82\n"
    "v_dot2c_f32_bf16 v93, v112, v82\n"
    "v_dot2c_f32_bf16 v100, v108, v90\n"
    "v_dot2c_f32_bf16 v101, v112, v90\n"
    "v_dot2c_f32_bf16 v94, v109, v82\n"
    "v_dot2c_f32_bf16 v95, v113, v82\n"
    "v_dot2c_f32_bf16 v102, v109, v90\n"
    "v_dot2c_f32_bf16 v103, v113, v90\n"
    "v_dot2c_f32_bf16 v96, v110, v82\n"
    "v_dot2c_f32_bf16 v97, v114, v82\n"
    "v_dot2c_f32_bf16 v104, v110, v90\n"
    "v_dot2c_f32_bf16 v105, v114, v90\n"
    "v_dot2c_f32_bf16 v98, v111, v82\n"
    "v_dot2c_f32_bf16 v99, v115, v82\n"
    "v_dot2c_f32_bf16 v106, v111, v90\n"
    "v_dot2c_f32_bf16 v107, v115, v90\n"
    "s_waitcnt vmcnt(0)\n"
    "v_perm_b32 v108, v72, v68, s40\n"
    "v_perm_b32 v112, v72, v68, s41\n"
    "v_perm_b32 v109, v73, v69, s40\n"
    "v_perm_b32 v113, v73, v69, s41\n"
    "v_perm_b32 v110, v74, v70, s40\n"
    "v_perm_b32 v114, v74, v70, s41\n"
    "v_perm_b32 v111, v75, v71, s40\n"
    "v_perm_b32 v115, v75, v71, s41\n"
    "v_dot2c_f32_bf16 v92, v108, v83\n"
    "v_dot2c_f32_bf16 v93, v112, v83\n"
    "v_dot2c_f32_bf16 v100, v108, v91\n"
    "v_dot2c_f32_bf16 v101, v112, v91\n"
    "v_dot2c_f32_bf16 v94, v109, v83\n"
    "v_dot2c_f32_bf16 v95, v113, v83\n"
    "v_dot2c_f32_bf16 v102, v109, v91\n"
    "v_dot2c_f32_bf16 v103, v113, v91\n"
    "v_dot2c_f32_bf16 v96, v110, v83\n"
    "v_dot2c_f32_bf16 v97, v114, v83\n"
    "v_dot2c_f32_bf16 v104, v110, v91\n"
    "v_dot2c_f32_bf16 v105, v114, v91\n"
    "v_dot2c_f32_bf16 v98, v111, v83\n"
    "v_dot2c_f32_bf16 v99, v115, v83\n"
    "v_dot2c_f32_bf16 v106, v111, v91\n"
    "v_dot2c_f32_bf16 v107, v115, v91\n"
    "v_permlane32_swap_b32 v92, v100\n"
    "v_permlane32_swap_b32 v93, v101\n"
    "v_permlane32_swap_b32 v94, v102\n"
    "v_permlane32_swap_b32 v95, v103\n"
    "v_permlane32_swap_b32 v96, v104\n"
    "v_permlane32_swap_b32 v97, v105\n"
    "v_permlane32_swap_b32 v98, v106\n"
    "v_permlane32_swap_b32 v99, v107\n"
    "v_pk_add_f32 v[92:93], v[92:93], v[100:101]\n"
    "v_pk_add_f32 v[94:95], v[94:95], v[102:103]\n"
    "v_pk_add_f32 v[96:97], v[96:97], v[104:105]\n"
    "v_pk_add_f32 v[98:99], v[98:99], v[106:107]\n"
    "s_nop 0\n"
    "v_permlane16_swap_b32 v92, v96\n"
    "v_permlane16_swap_b32 v93, v97\n"
    "v_permlane16_swap_b32 v94, v98\n"
    "v_permlane16_swap_b32 v95, v99\n"
    "v_pk_add_f32 v[92:93], v[92:93], v[96:97]\n"
    "v_pk_add_f32 v[94:95], v[94:95], v[98:99]\n"
    "s_nop 0\n"
    "v_add_f32_dpp v92, v92, v92 row_ror:8 row_mask:0xf bank_mask:0xf bound_ctrl:1\n"
    "v_add_f32_dpp v93, v93, v93 row_ror:8 row_mask:0xf bank_mask:0xf bound_ctrl:1\n"
    "v_add_f32_dpp v94, v94, v94 row_ror:8 row_mask:0xf bank_mask:0xf bound_ctrl:1\n"
    "v_add_f32_dpp v95, v95, v95 row_ror:8 row_mask:0xf bank_mask:0xf bound_ctrl:1\n"
    "v_cndmask_b32 v92, v92, v116, s[44:45]\n"
    "v_cndmask_b32 v93, v93, v117, s[44:45]\n"
    "v_and_or_b32 v100, v92, v6, s56\n"
    "v_and_or_b32 v101, v93, v6, s57\n"
    "v_and_or_b32 v102, v94, v6, s58\n"
    "v_and_or_b32 v103, v95, v6, s59\n"
    "buffer_store_dwordx4 v[100:103], v9, s[36:39], 0 offen offset:0 sc1\n"
    "s_mov_b64 exec, 1\n"
    "ds_add_u32 v5, v4 offset:3028\n"
    "s_mov_b64 exec, -1\n"
    "s_endpgm\n"
    ".LATs_fused_decode:\n"
    "s_setpc_b64 s[48:49]\n"
    ".LAT_fused_decode:\n"
    "v_lshlrev_b32 v104, 4, v105\n"
    "v_lshlrev_b32 v105, 3, v105\n"
    "v_mov_b32 v36, 0\n"
    "v_mov_b32 v37, 0\n"
    "v_mov_b32 v38, 0\n"
    "v_mov_b32 v39, 0\n"
    "v_mov_b32 v40, 0\n"
    "v_mov_b32 v41, 0\n"
    "v_mov_b32 v42, 0\n"
    "v_mov_b32 v43, 0\n"
    "v_mov_b32 v44, 0\n"
    "v_mov_b32 v45, 0\n"
    "v_mov_b32 v46, 0\n"
    "v_mov_b32 v47, 0\n"
    "v_mov_b32 v48, 0\n"
    "v_mov_b32 v49, 0\n"
    "v_mov_b32 v50, 0\n"
    "v_mov_b32 v51, 0\n"
    "s_waitcnt vmcnt(11)\n"
    "v_perm_b32 v12, v112, v108, s40\n"
    "v_perm_b32 v112, v112, v108, s41\n"
    "v_perm_b32 v13, v113, v109, s40\n"
    "v_perm_b32 v113, v113, v109, s41\n"
    "v_perm_b32 v14, v114, v110, s40\n"
    "v_perm_b32 v114, v114, v110, s41\n"
    "v_perm_b32 v15, v115, v111, s40\n"
    "v_perm_b32 v115, v115, v111, s41\n"
    "s_waitcnt vmcnt(9)\n"
    "v_perm_b32 v16, v120, v116, s40\n"
    "v_perm_b32 v120, v120, v116, s41\n"
    "v_perm_b32 v17, v121, v117, s40\n"
    "v_perm_b32 v121, v121, v117, s41\n"
    "v_perm_b32 v18, v122, v118, s40\n"
    "v_perm_b32 v122, v122, v118, s41\n"
    "v_perm_b32 v19, v123, v119, s40\n"
    "v_perm_b32 v123, v123, v119, s41\n"
    "s_waitcnt vmcnt(7)\n"
    "v_perm_b32 v20, v128, v124, s40\n"
    "v_perm_b32 v128, v128, v124, s41\n"
    "v_perm_b32 v21, v129, v125, s40\n"
    "v_perm_b32 v129, v129, v125, s41\n"
    "v_perm_b32 v22, v130, v126, s40\n"
    "v_perm_b32 v130, v130, v126, s41\n"
    "v_perm_b32 v23, v131, v127, s40\n"
    "v_perm_b32 v131, v131, v127, s41\n"
    "s_waitcnt vmcnt(5)\n"
    "v_perm_b32 v24, v136, v132, s40\n"
    "v_perm_b32 v136, v136, v132, s41\n"
    "v_perm_b32 v25, v137, v133, s40\n"
    "v_perm_b32 v137, v137, v133, s41\n"
    "v_perm_b32 v26, v138, v134, s40\n"
    "v_perm_b32 v138, v138, v134, s41\n"
    "v_perm_b32 v27, v139, v135, s40\n"
    "v_perm_b32 v139, v139, v135, s41\n"
    "s_waitcnt vmcnt(3)\n"
    "v_perm_b32 v28, v144, v140, s40\n"
    "v_perm_b32 v144, v144, v140, s41\n"
    "v_perm_b32 v29, v145, v141, s40\n"
    "v_perm_b32 v145, v145, v141, s41\n"
    "v_perm_b32 v30, v146, v142, s40\n"
    "v_perm_b32 v146, v146, v142, s41\n"
    "v_perm_b32 v31, v147, v143, s40\n"
    "v_perm_b32 v147, v147, v143, s41\n"
    "s_waitcnt vmcnt(1)\n"
    "v_perm_b32 v32, v152, v148, s40\n"
    "v_perm_b32 v152, v152, v148, s41\n"
    "v_perm_b32 v33, v153, v149, s40\n"
    "v_perm_b32 v153, v153, v149, s41\n"
    "v_perm_b32 v34, v154, v150, s40\n"
    "v_perm_b32 v154, v154, v150, s41\n"
    "v_perm_b32 v35, v155, v151, s40\n"
    "v_perm_b32 v155, v155, v151, s41\n"
    "s_setprio 0\n"
    "s_cmp_eq_u32 s26, 7\n"
    "s_cbranch_scc1 .LNH_fused_decode\n"
    ".LW6_fused_decode:\n"
    "ds_read_b32 v7, v5 offset:3048\n"
    "ds_read_b64 v[56:57], v105 offset:2688\n"
    "s_waitcnt lgkmcnt(0)\n"
    "v_cmp_eq_u32 vcc, 0, v7\n"
    "s_nop 0\n"
    "s_cbranch_vccnz .LW6_fused_decode\n"
    "v_dot2c_f32_bf16 v44, v28, v56\n"
    "v_dot2c_f32_bf16 v45, v144, v56\n"
    "v_dot2c_f32_bf16 v46, v29, v56\n"
    "v_dot2c_f32_bf16 v47, v145, v56\n"
    "v_dot2c_f32_bf16 v48, v30, v56\n"
    "v_dot2c_f32_bf16 v49, v146, v56\n"
    "v_dot2c_f32_bf16 v50, v31, v56\n"
    "v_dot2c_f32_bf16 v51, v147, v56\n"
    "v_dot2c_f32_bf16 v44, v32, v57\n"
    "v_dot2c_f32_bf16 v45, v152, v57\n"
    "v_dot2c_f32_bf16 v46, v33, v57\n"
    "v_dot2c_f32_bf16 v47, v153, v57\n"
    "v_dot2c_f32_bf16 v48, v34, v57\n"
    "v_dot2c_f32_bf16 v49, v154, v57\n"
    "v_dot2c_f32_bf16 v50, v35, v57\n"
    "v_dot2c_f32_bf16 v51, v155, v57\n"
    ".LNH_fused_decode:\n"
    ".LW7_fused_decode:\n"
    "ds_read_b32 v7, v5 offset:3044\n"
    "ds_read_b128 v[52:55], v104 offset:2656\n"
    "s_waitcnt lgkmcnt(0)\n"
    "v_cmp_eq_u32 vcc, 0, v7\n"
    "s_nop 0\n"
    "s_cbranch_vccnz .LW7_fused_decode\n"
    "s_setprio 3\n"
    "v_dot2c_f32_bf16 v36, v12, v52\n"
    "v_dot2c_f32_bf16 v37, v112, v52\n"
    "v_dot2c_f32_bf16 v38, v13, v52\n"
    "v_dot2c_f32_bf16 v39, v113, v52\n"
    "v_dot2c_f32_bf16 v40, v14, v52\n"
    "v_dot2c_f32_bf16 v41, v114, v52\n"
    "v_dot2c_f32_bf16 v42, v15, v52\n"
    "v_dot2c_f32_bf16 v43, v115, v52\n"
    "v_dot2c_f32_bf16 v36, v16, v53\n"
    "v_dot2c_f32_bf16 v37, v120, v53\n"
    "v_dot2c_f32_bf16 v38, v17, v53\n"
    "v_dot2c_f32_bf16 v39, v121, v53\n"
    "v_dot2c_f32_bf16 v40, v18, v53\n"
    "v_dot2c_f32_bf16 v41, v122, v53\n"
    "v_dot2c_f32_bf16 v42, v19, v53\n"
    "v_dot2c_f32_bf16 v43, v123, v53\n"
    "v_dot2c_f32_bf16 v36, v20, v54\n"
    "v_dot2c_f32_bf16 v37, v128, v54\n"
    "v_dot2c_f32_bf16 v38, v21, v54\n"
    "v_dot2c_f32_bf16 v39, v129, v54\n"
    "v_dot2c_f32_bf16 v40, v22, v54\n"
    "v_dot2c_f32_bf16 v41, v130, v54\n"
    "v_dot2c_f32_bf16 v42, v23, v54\n"
    "v_dot2c_f32_bf16 v43, v131, v54\n"
    "v_dot2c_f32_bf16 v36, v24, v55\n"
    "v_dot2c_f32_bf16 v37, v136, v55\n"
    "v_dot2c_f32_bf16 v38, v25, v55\n"
    "v_dot2c_f32_bf16 v39, v137, v55\n"
    "v_dot2c_f32_bf16 v40, v26, v55\n"
    "v_dot2c_f32_bf16 v41, v138, v55\n"
    "v_dot2c_f32_bf16 v42, v27, v55\n"
    "v_dot2c_f32_bf16 v43, v139, v55\n"
    "v_permlane32_swap_b32 v36, v44\n"
    "v_permlane32_swap_b32 v37, v45\n"
    "v_permlane32_swap_b32 v38, v46\n"
    "v_permlane32_swap_b32 v39, v47\n"
    "v_pk_add_f32 v[36:37], v[36:37], v[44:45]\n"
    "v_pk_add_f32 v[38:39], v[38:39], v[46:47]\n"
    "v_and_or_b32 v60, v36, v6, s56\n"
    "v_and_or_b32 v61, v37, v6, s57\n"
    "v_and_or_b32 v62, v38, v6, s58\n"
    "v_and_or_b32 v63, v39, v6, s59\n"
    "v_permlane32_swap_b32 v40, v48\n"
    "v_permlane32_swap_b32 v41, v49\n"
    "v_permlane32_swap_b32 v42, v50\n"
    "v_permlane32_swap_b32 v43, v51\n"
    "v_pk_add_f32 v[40:41], v[40:41], v[48:49]\n"
    "v_pk_add_f32 v[42:43], v[42:43], v[50:51]\n"
    "v_and_or_b32 v64, v40, v6, s56\n"
    "v_and_or_b32 v65, v41, v6, s57\n"
    "v_and_or_b32 v66, v42, v6, s58\n"
    "v_and_or_b32 v67, v43, v6, s59\n"
    "v_permlane16_swap_b32 v60, v64\n"
    "v_permlane16_swap_b32 v61, v65\n"
    "v_permlane16_swap_b32 v62, v66\n"
    "v_permlane16_swap_b32 v63, v67\n"
    "buffer_store_dwordx4 v[60:63], v107, s[36:39], 0 offen offset:0 sc1\n"
    "buffer_store_dwordx4 v[64:67], v156, s[36:39], 0 offen offset:0 sc1\n"
    ".LEXIT_fused_decode:\n"
    "s_endpgm\n"
    ".LFINs_fused_decode:\n"
    "s_setpc_b64 s[48:49]\n"
    ".LFIN_fused_decode:\n"
    "s_lshl_b32 s42, s16, 14\n"
    "s_lshl_b32 s43, s25, 12\n"
    "s_add_u32 s42, s42, s43\n"
    "s_add_u32 s42, s42, 872448\n"
    "v_lshl_add_u32 v8, v1, 4, s42\n"
    "v_and_b32 v28, 3, v1\n"
    "v_lshlrev_b32 v28, 4, v28\n"
    "v_lshrrev_b32 v29, 4, v1\n"
    "v_lshl_add_u32 v28, v29, 2, v28\n"
    "s_lshl_b32 s42, s25, 6\n"
    "v_add_u32 v28, s42, v28\n"
    "v_lshlrev_b32 v29, 2, v3\n"
    "v_add_u32 v29, 2752, v29\n"
    "s_setprio 0\n"
    ".LW28_fused_decode:\n"
    "ds_read_b32 v7, v5 offset:3040\n"
    "s_waitcnt lgkmcnt(0)\n"
    "v_cmp_gt_u32 vcc, 1, v7\n"
    "s_nop 0\n"
    "s_cbranch_vccz .LW28_fused_decode_d\n"
    "s_sleep 1\n"
    "s_branch .LW28_fused_decode\n"
    ".LW28_fused_decode_d:\n"
    "s_cmp_eq_u32 s25, 2\n"
    "s_cbranch_scc0 .LNO_fused_decode\n"
    "global_load_ushort v33, v30, s[22:23]\n"
    ".LNO_fused_decode:\n"
    "s_mov_b32 s34, 131072\n"
    ".LG29_fused_decode:\n"
    "buffer_load_dwordx4 v[68:71], v26, s[36:39], 0 offen offset:0 sc1\n"
    "s_waitcnt vmcnt(0)\n"
    "v_xor_b32 v68, s56, v68\n"
    "v_xor_b32 v69, s57, v69\n"
    "v_xor_b32 v70, s58, v70\n"
    "v_xor_b32 v71, s59, v71\n"
    "v_or3_b32 v7, v68, v69, v70\n"
    "v_or_b32 v7, v7, v71\n"
    "v_and_b32 v7, 0xfff, v7\n"
    "v_cmp_ne_u32 vcc, 0, v7\n"
    "s_nop 0\n"
    "s_cbranch_vccz .LG29_fused_decode_d\n"
    "s_mov_b64 exec, vcc\n"
    "s_sleep 4\n"
    "s_sub_u32 s34, s34, 1\n"
    "s_cbranch_scc0 .LG29_fused_decode\n"
    ".LG29_fused_decode_d:\n"
    "s_mov_b64 exec, -1\n"
    "s_memrealtime s[46:47]\n"
    "v_lshlrev_b32 v31, 16, v31\n"
    "s_waitcnt lgkmcnt(0)\n"
    "s_lshl_b32 s43, s46, 12\n"
    "v_subrev_u32 v68, s43, v68\n"
    "s_nop 1\n"
    "v_max_i32_dpp v68, v68, v68 quad_perm:[1,0,3,2] row_mask:0xf bank_mask:0xf bound_ctrl:1\n"
    "s_nop 1\n"
    "v_max_i32_dpp v68, v68, v68 quad_perm:[2,3,0,1] row_mask:0xf bank_mask:0xf bound_ctrl:1\n"
    "s_nop 1\n"
    "v_max_i32_dpp v68, v68, v68 row_half_mirror row_mask:0xf bank_mask:0xf bound_ctrl:1\n"
    "s_nop 0\n"
    "v_readfirstlane_b32 s43, v68\n"
    "s_nop 0\n"
    "s_ashr_i32 s43, s43, 12\n"
    "s_add_u32 s46, s46, s43\n"
    "s_add_u32 s46, s46, 184\n"
    ".LT30_fused_decode:\n"
    "s_memrealtime s[48:49]\n"
    "s_waitcnt lgkmcnt(0)\n"
    "s_sub_u32 s43, s48, s46\n"
    "s_bitcmp1_b32 s43, 19\n"
    "s_cbranch_scc1 .LT30_fused_decode\n"
    "s_setprio 3\n"
    "s_mov_b32 s34, 131072\n"
    ".LG31_fused_decode:\n"
    "buffer_load_dwordx4 v[36:39], v8, s[36:39], 0 offen offset:0 sc1\n"
    "buffer_load_dwordx4 v[40:43], v8, s[36:39], 0 offen offset:1024 sc1\n"
    "buffer_load_dwordx4 v[44:47], v8, s[36:39], 0 offen offset:2048 sc1\n"
    "buffer_load_dwordx4 v[48:51], v8, s[36:39], 0 offen offset:3072 sc1\n"
    "s_waitcnt vmcnt(3)\n"
    "v_xor_b32 v36, s56, v36\n"
    "v_xor_b32 v37, s57, v37\n"
    "v_xor_b32 v38, s58, v38\n"
    "v_xor_b32 v39, s59, v39\n"
    "v_or3_b32 v7, v36, v37, v38\n"
    "v_or_b32 v7, v7, v39\n"
    "s_waitcnt vmcnt(2)\n"
    "v_xor_b32 v40, s56, v40\n"
    "v_xor_b32 v41, s57, v41\n"
    "v_xor_b32 v42, s58, v42\n"
    "v_xor_b32 v43, s59, v43\n"
    "v_or3_b32 v7, v7, v40, v41\n"
    "v_or3_b32 v7, v7, v42, v43\n"
    "s_waitcnt vmcnt(1)\n"
    "v_xor_b32 v44, s56, v44\n"
    "v_xor_b32 v45, s57, v45\n"
    "v_xor_b32 v46, s58, v46\n"
    "v_xor_b32 v47, s59, v47\n"
    "v_or3_b32 v7, v7, v44, v45\n"
    "v_or3_b32 v7, v7, v46, v47\n"
    "s_waitcnt vmcnt(0)\n"
    "v_xor_b32 v48, s56, v48\n"
    "v_xor_b32 v49, s57, v49\n"
    "v_xor_b32 v50, s58, v50\n"
    "v_xor_b32 v51, s59, v51\n"
    "v_or3_b32 v7, v7, v48, v49\n"
    "v_or3_b32 v7, v7, v50, v51\n"
    "v_and_b32 v7, 0xfff, v7\n"
    "v_cmp_ne_u32 vcc, 0, v7\n"
    "s_nop 0\n"
    "s_cbranch_vccz .LG31_fused_decode_d\n"
    "s_mov_b64 exec, vcc\n"
    "s_sub_u32 s34, s34, 1\n"
    "s_cbranch_scc0 .LG31_fused_decode\n"
    ".LG31_fused_decode_d:\n"
    "s_mov_b64 exec, -1\n"
    "v_pk_add_f32 v[36:37], v[36:37], v[40:41]\n"
    "v_pk_add_f32 v[38:39], v[38:39], v[42:43]\n"
    "v_pk_add_f32 v[44:45], v[44:45], v[48:49]\n"
    "v_pk_add_f32 v[46:47], v[46:47], v[50:51]\n"
    "v_pk_add_f32 v[36:37], v[36:37], v[44:45]\n"
    "v_pk_add_f32 v[38:39], v[38:39], v[46:47]\n"
    "s_nop 1\n"
    "v_permlane32_swap_b32 v36, v38\n"
    "v_permlane32_swap_b32 v37, v39\n"
    "v_pk_add_f32 v[36:37], v[36:37], v[38:39]\n"
    "s_nop 1\n"
    "v_permlane16_swap_b32 v36, v37\n"
    "v_add_f32 v36, v36, v37\n"
    "s_nop 1\n"
    "v_add_f32_dpp v36, v36, v36 row_ror:8 row_mask:0xf bank_mask:0xf bound_ctrl:1\n"
    "s_nop 1\n"
    "v_add_f32_dpp v36, v36, v36 row_ror:4 row_mask:0xf bank_mask:0xf bound_ctrl:1\n"
    "ds_write_b32 v28, v36 offset:2752\n"
    "s_mov_b64 exec, 1\n"
    "ds_add_rtn_u32 v7, v5, v4 offset:3052\n"
    "s_waitcnt lgkmcnt(0)\n"
    "v_readfirstlane_b32 s42, v7\n"
    "s_mov_b64 exec, -1\n"
    "s_cmp_eq_u32 s42, 3\n"
    "s_cbranch_scc0 .LEX2_fused_decode\n"
    "ds_read2_b32 v[40:41], v29 offset0:8 offset1:24\n"
    "ds_read2_b32 v[42:43], v29 offset0:40 offset1:56\n"
    "ds_read2_b32 v[36:37], v29 offset0:0 offset1:16\n"
    "ds_read2_b32 v[38:39], v29 offset0:32 offset1:48\n"
    "s_waitcnt lgkmcnt(2)\n"
    "v_add_f32 v40, v40, v41\n"
    "v_add_f32 v42, v42, v43\n"
    "v_add_f32 v40, v40, v42\n"
    "v_cvt_pk_bf16_f32 v40, v40, v40\n"
    "v_lshlrev_b32 v32, 16, v40\n"
    "v_mul_f32 v34, 0xbfb8aa3b, v32\n"
    "v_exp_f32 v34, v34\n"
    "s_waitcnt lgkmcnt(0)\n"
    "v_add_f32 v36, v36, v37\n"
    "v_add_f32 v34, 1.0, v34\n"
    "v_add_f32 v38, v38, v39\n"
    "v_rcp_f32 v34, v34\n"
    "v_add_f32 v36, v36, v38\n"
    "v_mul_f32 v32, v32, v34\n"
    "v_cvt_pk_bf16_f32 v36, v36, v32\n"
    "v_and_b32 v32, 0xffff0000, v36\n"
    "v_lshlrev_b32 v35, 16, v36\n"
    "v_mul_f32 v35, v35, v32\n"
    "v_cvt_pk_bf16_f32 v35, v35, v35\n"
    "v_lshlrev_b32 v35, 16, v35\n"
    "v_add_f32 v35, v31, v35\n"
    "v_cvt_pk_bf16_f32 v35, v35, v35\n"
    "s_mov_b64 exec, 0xff\n"
    "global_store_short v30, v35, s[22:23] sc1\n"
    "s_cmp_eq_u32 s16, 0\n"
    "s_cbranch_scc0 .LEX2_fused_decode\n"
    "s_mov_b64 exec, 15\n"
    "v_lshlrev_b32 v40, 4, v1\n"
    "v_mov_b32 v36, s56\n"
    "v_mov_b32 v37, s57\n"
    "v_mov_b32 v38, s58\n"
    "v_mov_b32 v39, s59\n"
    "buffer_store_dwordx4 v[36:39], v40, s[36:39], 0 offen offset:0 sc1\n"
    ".LEX2_fused_decode:\n"
    "s_endpgm\n"
    ".p2align 12\n"
    ".LV_fused_decode:\n"
    "s_setprio 1\n"
    "s_sub_u32 s25, s24, 6\n"
    "s_lshl_b32 s43, s25, 7\n"
    "v_lshlrev_b32 v8, 9, v2\n"
    "v_lshl_add_u32 v8, v3, 4, v8\n"
    "v_add_u32 v8, s43, v8\n"
    "s_waitcnt lgkmcnt(0)\n"
    "s_add_u32 s28, s10, s33\n"
    "s_addc_u32 s29, s11, 0\n"
    "s_and_b32 s29, s29, s53\n"
    "s_mov_b32 s36, s8\n"
    "s_and_b32 s37, s9, 0xffff\n"
    "s_mul_i32 s44, s25, 320\n"
    "s_add_u32 s43, s35, s44\n"
    "s_add_u32 s43, s43, 768\n"
    "v_lshrrev_b32 v9, 5, v1\n"
    "v_mul_u32_u24 v9, 1280, v9\n"
    "v_lshrrev_b32 v10, 1, v3\n"
    "v_mul_u32_u24 v10, 80, v10\n"
    "v_add3_u32 v9, v9, v10, s43\n"
    "v_and_b32 v10, 1, v1\n"
    "v_lshlrev_b32 v10, 5, v10\n"
    "v_and_b32 v11, 16, v1\n"
    "v_add_u32 v10, v10, v11\n"
    "v_and_b32 v11, 0x19, v1\n"
    "v_cmp_eq_u32 vcc, 8, v11\n"
    "s_nop 1\n"
    "v_cndmask_b32 v10, v10, 64, vcc\n"
    "v_add_u32 v9, v9, v10\n"
    "v_and_b32 v11, 8, v1\n"
    "v_lshlrev_b32 v11, 28, v11\n"
    "v_cndmask_b32 v11, v11, 0, vcc\n"
    "v_or_b32 v9, v9, v11\n"
    "s_mov_b64 s[44:45], vcc\n"
    "v_lshlrev_b32 v10, 6, v2\n"
    "v_lshrrev_b32 v11, 5, v1\n"
    "v_lshlrev_b32 v11, 2, v11\n"
    "v_add_u32 v11, 2560, v11\n"
    "v_mov_b32 v92, 0\n"
    "v_mov_b32 v93, 0\n"
    "v_mov_b32 v94, 0\n"
    "v_mov_b32 v95, 0\n"
    "v_mov_b32 v96, 0\n"
    "v_mov_b32 v97, 0\n"
    "v_mov_b32 v98, 0\n"
    "v_mov_b32 v99, 0\n"
    "v_mov_b32 v100, 0\n"
    "v_mov_b32 v101, 0\n"
    "v_mov_b32 v102, 0\n"
    "v_mov_b32 v103, 0\n"
    "v_mov_b32 v104, 0\n"
    "v_mov_b32 v105, 0\n"
    "v_mov_b32 v106, 0\n"
    "v_mov_b32 v107, 0\n"
    "s_cmp_eq_u32 s25, 0\n"
    "s_cbranch_scc0 .LC13_fused_decode\n"
    "s_call_b64 s[48:49], .LA0Ts_fused_decode\n"
    ".LC13_fused_decode:\n"
    "s_cmp_eq_u32 s25, 1\n"
    "s_cbranch_scc0 .LC14_fused_decode\n"
    "s_call_b64 s[48:49], .LVBs_fused_decode\n"
    ".LC14_fused_decode:\n"
    "s_cmp_eq_u32 s25, 2\n"
    "s_cbranch_scc0 .LC15_fused_decode\n"
    "s_call_b64 s[48:49], .LATs_fused_decode\n"
    ".LC15_fused_decode:\n"
    "s_cmp_eq_u32 s25, 3\n"
    "s_cbranch_scc0 .LC16_fused_decode\n"
    "s_call_b64 s[48:49], .LFINs_fused_decode\n"
    ".LC16_fused_decode:\n"
    ".LW17_fused_decode:\n"
    "ds_read_b32 v7, v5 offset:3012\n"
    "s_waitcnt lgkmcnt(0)\n"
    "v_cmp_gt_u32 vcc, 4, v7\n"
    "s_nop 0\n"
    "s_cbranch_vccz .LW17_fused_decode_d\n"
    "s_sleep 1\n"
    "s_branch .LW17_fused_decode\n"
    ".LW17_fused_decode_d:\n"
    "s_cmp_eq_u32 s25, 0\n"
    "s_cbranch_scc0 .LNS_fused_decode\n"
    "s_memrealtime s[46:47]\n"
    "s_waitcnt lgkmcnt(0)\n"
    "v_mov_b32 v12, s46\n"
    "ds_write_b32 v5, v12 offset:2644\n"
    ".LNS_fused_decode:\n"
    "buffer_load_dwordx4 v[12:15], v8, s[28:31], s42 offen nt\n"
    "s_add_u32 s42, s42, s55\n"
    "s_and_b32 s42, s42, s53\n"
    "s_sleep 5\n"
    "buffer_load_dwordx4 v[16:19], v8, s[28:31], s42 offen nt\n"
    "s_add_u32 s42, s42, s55\n"
    "s_and_b32 s42, s42, s53\n"
    "s_sleep 5\n"
    "buffer_load_dwordx4 v[20:23], v8, s[28:31], s42 offen nt\n"
    "s_add_u32 s42, s42, s55\n"
    "s_and_b32 s42, s42, s53\n"
    "s_sleep 5\n"
    "buffer_load_dwordx4 v[24:27], v8, s[28:31], s42 offen nt\n"
    "s_add_u32 s42, s42, s55\n"
    "s_and_b32 s42, s42, s53\n"
    "s_sleep 5\n"
    "buffer_load_dwordx4 v[28:31], v8, s[28:31], s42 offen nt\n"
    "s_add_u32 s42, s42, s55\n"
    "s_and_b32 s42, s42, s53\n"
    "s_sleep 5\n"
    "buffer_load_dwordx4 v[32:35], v8, s[28:31], s42 offen nt\n"
    "s_add_u32 s42, s42, s55\n"
    "s_and_b32 s42, s42, s53\n"
    "s_sleep 5\n"
    "buffer_load_dwordx4 v[36:39], v8, s[28:31], s42 offen nt\n"
    "s_add_u32 s42, s42, s55\n"
    "s_and_b32 s42, s42, s53\n"
    "s_waitcnt vmcnt(6)\n"
    "buffer_load_dwordx4 v[40:43], v8, s[28:31], s42 offen nt\n"
    "s_add_u32 s42, s42, s55\n"
    "s_and_b32 s42, s42, s53\n"
    "s_mov_b64 exec, 1\n"
    "ds_add_u32 v5, v4 offset:3036\n"
    "s_mov_b64 exec, -1\n"
    "s_waitcnt vmcnt(6)\n"
    "buffer_load_dwordx4 v[44:47], v8, s[28:31], s42 offen nt\n"
    "s_add_u32 s42, s42, s55\n"
    "s_and_b32 s42, s42, s53\n"
    "s_waitcnt vmcnt(6)\n"
    "buffer_load_dwordx4 v[48:51], v8, s[28:31], s42 offen nt\n"
    "s_add_u32 s42, s42, s55\n"
    "s_and_b32 s42, s42, s53\n"
    "s_waitcnt vmcnt(6)\n"
    "buffer_load_dwordx4 v[52:55], v8, s[28:31], s42 offen nt\n"
    "s_add_u32 s42, s42, s55\n"
    "s_and_b32 s42, s42, s53\n"
    "s_waitcnt vmcnt(6)\n"
    "buffer_load_dwordx4 v[56:59], v8, s[28:31], s42 offen nt\n"
    "s_add_u32 s42, s42, s55\n"
    "s_and_b32 s42, s42, s53\n"
    "s_waitcnt vmcnt(6)\n"
    "buffer_load_dwordx4 v[60:63], v8, s[28:31], s42 offen nt\n"
    "s_add_u32 s42, s42, s55\n"
    "s_and_b32 s42, s42, s53\n"
    "s_waitcnt vmcnt(6)\n"
    "buffer_load_dwordx4 v[64:67], v8, s[28:31], s42 offen nt\n"
    "s_add_u32 s42, s42, s55\n"
    "s_and_b32 s42, s42, s53\n"
    "s_waitcnt vmcnt(6)\n"
    "buffer_load_dwordx4 v[68:71], v8, s[28:31], s42 offen nt\n"
    "s_add_u32 s42, s42, s55\n"
    "s_and_b32 s42, s42, s53\n"
    "s_mov_b64 exec, 1\n"
    "ds_add_u32 v5, v4 offset:3032\n"
    "s_mov_b64 exec, -1\n"
    "s_waitcnt vmcnt(6)\n"
    "buffer_load_dwordx4 v[72:75], v8, s[28:31], s42 offen nt\n"
    "s_mov_b64 exec, 1\n"
    "ds_add_u32 v5, v4 offset:3016\n"
    "s_mov_b64 exec, -1\n"
    "s_branch .LVB_fused_decode\n"
    ".LA1B_fused_decode:\n"
    "s_setprio 3\n"
    "s_load_dwordx2 s[18:19], s[0:1], 0x38\n"
    "s_load_dwordx4 s[20:23], s[0:1], 0x40\n"
    "s_waitcnt lgkmcnt(0)\n"
    "s_mov_b32 s36, s8\n"
    "s_and_b32 s37, s9, 0xffff\n"
    "s_and_b32 s33, s27, 7\n"
    "s_mul_i32 s42, s33, 200704\n"
    "s_lshr_b32 s43, s27, 3\n"
    "s_lshl_b32 s44, s26, 2\n"
    "s_add_u32 s43, s43, s44\n"
    "s_lshl_b32 s43, s43, 7\n"
    "s_add_u32 s42, s42, s43\n"
    "v_mul_u32_u24 v9, 7168, v2\n"
    "v_lshl_add_u32 v9, v3, 4, v9\n"
    "v_add_u32 v9, s42, v9\n"
    "v_bfrev_b32 v11, 1\n"
    "v_cmp_gt_u32 vcc, 32, v1\n"
    "s_nop 1\n"
    "v_cndmask_b32 v10, v11, v9, vcc\n"
    "s_cmp_eq_u32 s26, 7\n"
    "s_cselect_b32 s30, 0, 1605632\n"
    "s_mov_b32 s46, s30\n"
    "s_mov_b32 s47, s31\n"
    "s_mov_b32 s28, s12\n"
    "s_and_b32 s29, s13, 0xffff\n"
    "s_mov_b32 s44, s14\n"
    "s_and_b32 s45, s15, 0xffff\n"
    "s_mul_i32 s42, s33, 112\n"
    "v_lshl_add_u32 v8, v2, 2, s42\n"
    "s_add_u32 s43, s35, 256\n"
    "s_cmp_eq_u32 s26, 7\n"
    "s_cselect_b32 s43, 0x80000000, s43\n"
    "v_lshrrev_b32 v11, 5, v1\n"
    "v_lshlrev_b32 v11, 8, v11\n"
    "v_lshl_add_u32 v11, v3, 5, v11\n"
    "v_and_b32 v7, 16, v1\n"
    "v_add3_u32 v11, v11, v7, s43\n"
    "v_and_b32 v7, 8, v1\n"
    "v_lshl_or_b32 v11, v7, 28, v11\n"
    "s_lshl_b32 s42, s26, 5\n"
    "s_add_u32 s42, s42, s27\n"
    "s_mul_i32 s43, s42, 0x3800\n"
    "s_add_u32 s12, s18, s43\n"
    "s_addc_u32 s13, s19, 0\n"
    "s_and_b32 s13, s13, 0xffff\n"
    "s_mul_i32 s43, s42, 0x1c00\n"
    "s_add_u32 s8, s20, s43\n"
    "s_addc_u32 s9, s21, 0\n"
    "s_and_b32 s9, s9, 0xffff\n"
    "s_cmp_eq_u32 s26, 7\n"
    "s_cselect_b32 s10, 0, 0x1c00\n"
    "s_mov_b32 s11, s31\n"
    "s_lshl_b32 s42, s42, 6\n"
    "s_add_u32 s42, s42, 1396736\n"
    "v_and_b32 v104, 31, v1\n"
    "v_lshrrev_b32 v105, 5, v1\n"
    "v_mul_u32_u24 v106, 1792, v105\n"
    "v_lshl_add_u32 v106, v104, 4, v106\n"
    "v_and_b32 v107, 15, v1\n"
    "v_lshlrev_b32 v107, 14, v107\n"
    "v_and_b32 v7, 48, v1\n"
    "v_add3_u32 v107, v107, v7, s42\n"
    "v_add_u32 v156, 0x40000, v107\n"
    "v_add_u32 v106, 512, v106\n"
    "v_cmp_gt_u32 vcc, 24, v104\n"
    "v_bfrev_b32 v104, 1\n"
    "s_nop 0\n"
    "v_cndmask_b32 v106, v104, v106, vcc\n"
    "v_and_b32 v7, 8, v1\n"
    "v_lshl_or_b32 v156, v7, 28, v156\n"
    "v_mov_b32 v80, 0\n"
    "v_mov_b32 v81, 0\n"
    "v_mov_b32 v82, 0\n"
    "v_mov_b32 v83, 0\n"
    "v_mov_b32 v84, 0\n"
    "v_mov_b32 v85, 0\n"
    "v_mov_b32 v86, 0\n"
    "v_mov_b32 v87, 0\n"
    "v_mov_b32 v88, 0\n"
    "v_mov_b32 v89, 0\n"
    "v_mov_b32 v90, 0\n"
    "v_mov_b32 v91, 0\n"
    "v_mov_b32 v92, 0\n"
    "v_mov_b32 v93, 0\n"
    "v_mov_b32 v94, 0\n"
    "v_mov_b32 v95, 0\n"
    ".LW8_fused_decode:\n"
    "ds_read_b32 v7, v5 offset:3036\n"
    "s_waitcnt lgkmcnt(0)\n"
    "v_cmp_gt_u32 vcc, 4, v7\n"
    "s_nop 0\n"
    "s_cbranch_vccz .LW8_fused_decode_d\n"
    "s_sleep 1\n"
    "s_branch .LW8_fused_decode\n"
    ".LW8_fused_decode_d:\n"
    "buffer_load_dwordx4 v[12:15], v9, s[28:31], 0 offen offset:0 nt\n"
    "buffer_load_dwordx4 v[16:19], v9, s[28:31], 0 offen offset:3584 nt\n"
    "s_mov_b32 s43, 57344\n"
    "buffer_load_dwordx4 v[20:23], v9, s[28:31], s43 offen offset:0 nt\n"
    "buffer_load_dwordx4 v[24:27], v9, s[28:31], s43 offen offset:3584 nt\n"
    "s_mov_b32 s43, 114688\n"
    "buffer_load_dwordx4 v[28:31], v9, s[28:31], s43 offen offset:0 nt\n"
    "buffer_load_dwordx4 v[32:35], v9, s[28:31], s43 offen offset:3584 nt\n"
    "s_mov_b32 s43, 172032\n"
    "buffer_load_dwordx4 v[36:39], v10, s[28:31], s43 offen offset:0 nt\n"
    "buffer_load_dwordx4 v[40:43], v10, s[28:31], s43 offen offset:3584 nt\n"
    "buffer_load_dwordx4 v[44:47], v9, s[44:47], 0 offen offset:0 nt\n"
    "buffer_load_dwordx4 v[48:51], v9, s[44:47], 0 offen offset:3584 nt\n"
    "s_mov_b32 s43, 57344\n"
    "buffer_load_dwordx4 v[52:55], v9, s[44:47], s43 offen offset:0 nt\n"
    "buffer_load_dwordx4 v[56:59], v9, s[44:47], s43 offen offset:3584 nt\n"
    "s_mov_b32 s43, 114688\n"
    "buffer_load_dwordx4 v[60:63], v9, s[44:47], s43 offen offset:0 nt\n"
    "buffer_load_dwordx4 v[64:67], v9, s[44:47], s43 offen offset:3584 nt\n"
    "s_mov_b32 s43, 172032\n"
    "buffer_load_dwordx4 v[68:71], v10, s[44:47], s43 offen offset:0 nt\n"
    "buffer_load_dwordx4 v[72:75], v10, s[44:47], s43 offen offset:3584 nt\n"
    "s_setprio 0\n"
    ".LW9_fused_decode:\n"
    "ds_read_b32 v7, v5 offset:3060\n"
    "s_waitcnt lgkmcnt(0)\n"
    "v_cmp_gt_u32 vcc, 1, v7\n"
    "s_nop 0\n"
    "s_cbranch_vccz .LW9_fused_decode_d\n"
    "s_sleep 1\n"
    "s_branch .LW9_fused_decode\n"
    ".LW9_fused_decode_d:\n"
    "ds_read_b32 v76, v8 offset:0\n"
    "ds_read_b32 v77, v8 offset:32\n"
    "ds_read_b32 v78, v8 offset:64\n"
    "ds_read_b32 v79, v8 offset:96\n"
    "ds_read_b128 v[96:99], v5 offset:2624\n"
    "s_waitcnt lgkmcnt(0)\n"
    "v_readfirstlane_b32 s56, v96\n"
    "v_readfirstlane_b32 s57, v97\n"
    "v_readfirstlane_b32 s58, v98\n"
    "v_readfirstlane_b32 s59, v99\n"
    "s_waitcnt vmcnt(14)\n"
    "v_perm_b32 v96, v16, v12, s40\n"
    "v_perm_b32 v100, v16, v12, s41\n"
    "v_perm_b32 v97, v17, v13, s40\n"
    "v_perm_b32 v101, v17, v13, s41\n"
    "v_perm_b32 v98, v18, v14, s40\n"
    "v_perm_b32 v102, v18, v14, s41\n"
    "v_perm_b32 v99, v19, v15, s40\n"
    "v_perm_b32 v103, v19, v15, s41\n"
    "v_dot2c_f32_bf16 v80, v96, v76\n"
    "v_dot2c_f32_bf16 v81, v100, v76\n"
    "v_dot2c_f32_bf16 v82, v97, v76\n"
    "v_dot2c_f32_bf16 v83, v101, v76\n"
    "v_dot2c_f32_bf16 v84, v98, v76\n"
    "v_dot2c_f32_bf16 v85, v102, v76\n"
    "v_dot2c_f32_bf16 v86, v99, v76\n"
    "v_dot2c_f32_bf16 v87, v103, v76\n"
    "s_waitcnt vmcnt(12)\n"
    "v_perm_b32 v96, v24, v20, s40\n"
    "v_perm_b32 v100, v24, v20, s41\n"
    "v_perm_b32 v97, v25, v21, s40\n"
    "v_perm_b32 v101, v25, v21, s41\n"
    "v_perm_b32 v98, v26, v22, s40\n"
    "v_perm_b32 v102, v26, v22, s41\n"
    "v_perm_b32 v99, v27, v23, s40\n"
    "v_perm_b32 v103, v27, v23, s41\n"
    "v_dot2c_f32_bf16 v80, v96, v77\n"
    "v_dot2c_f32_bf16 v81, v100, v77\n"
    "v_dot2c_f32_bf16 v82, v97, v77\n"
    "v_dot2c_f32_bf16 v83, v101, v77\n"
    "v_dot2c_f32_bf16 v84, v98, v77\n"
    "v_dot2c_f32_bf16 v85, v102, v77\n"
    "v_dot2c_f32_bf16 v86, v99, v77\n"
    "v_dot2c_f32_bf16 v87, v103, v77\n"
    "s_waitcnt vmcnt(10)\n"
    "v_perm_b32 v96, v32, v28, s40\n"
    "v_perm_b32 v100, v32, v28, s41\n"
    "v_perm_b32 v97, v33, v29, s40\n"
    "v_perm_b32 v101, v33, v29, s41\n"
    "v_perm_b32 v98, v34, v30, s40\n"
    "v_perm_b32 v102, v34, v30, s41\n"
    "v_perm_b32 v99, v35, v31, s40\n"
    "v_perm_b32 v103, v35, v31, s41\n"
    "v_dot2c_f32_bf16 v80, v96, v78\n"
    "v_dot2c_f32_bf16 v81, v100, v78\n"
    "v_dot2c_f32_bf16 v82, v97, v78\n"
    "v_dot2c_f32_bf16 v83, v101, v78\n"
    "v_dot2c_f32_bf16 v84, v98, v78\n"
    "v_dot2c_f32_bf16 v85, v102, v78\n"
    "v_dot2c_f32_bf16 v86, v99, v78\n"
    "v_dot2c_f32_bf16 v87, v103, v78\n"
    "s_waitcnt vmcnt(8)\n"
    "v_perm_b32 v96, v40, v36, s40\n"
    "v_perm_b32 v100, v40, v36, s41\n"
    "v_perm_b32 v97, v41, v37, s40\n"
    "v_perm_b32 v101, v41, v37, s41\n"
    "v_perm_b32 v98, v42, v38, s40\n"
    "v_perm_b32 v102, v42, v38, s41\n"
    "v_perm_b32 v99, v43, v39, s40\n"
    "v_perm_b32 v103, v43, v39, s41\n"
    "v_dot2c_f32_bf16 v80, v96, v79\n"
    "v_dot2c_f32_bf16 v81, v100, v79\n"
    "v_dot2c_f32_bf16 v82, v97, v79\n"
    "v_dot2c_f32_bf16 v83, v101, v79\n"
    "v_dot2c_f32_bf16 v84, v98, v79\n"
    "v_dot2c_f32_bf16 v85, v102, v79\n"
    "v_dot2c_f32_bf16 v86, v99, v79\n"
    "v_dot2c_f32_bf16 v87, v103, v79\n"
    "s_waitcnt vmcnt(6)\n"
    "v_perm_b32 v96, v48, v44, s40\n"
    "v_perm_b32 v100, v48, v44, s41\n"
    "v_perm_b32 v97, v49, v45, s40\n"
    "v_perm_b32 v101, v49, v45, s41\n"
    "v_perm_b32 v98, v50, v46, s40\n"
    "v_perm_b32 v102, v50, v46, s41\n"
    "v_perm_b32 v99, v51, v47, s40\n"
    "v_perm_b32 v103, v51, v47, s41\n"
    "v_dot2c_f32_bf16 v88, v96, v76\n"
    "v_dot2c_f32_bf16 v89, v100, v76\n"
    "v_dot2c_f32_bf16 v90, v97, v76\n"
    "v_dot2c_f32_bf16 v91, v101, v76\n"
    "v_dot2c_f32_bf16 v92, v98, v76\n"
    "v_dot2c_f32_bf16 v93, v102, v76\n"
    "v_dot2c_f32_bf16 v94, v99, v76\n"
    "v_dot2c_f32_bf16 v95, v103, v76\n"
    "s_waitcnt vmcnt(4)\n"
    "v_perm_b32 v96, v56, v52, s40\n"
    "v_perm_b32 v100, v56, v52, s41\n"
    "v_perm_b32 v97, v57, v53, s40\n"
    "v_perm_b32 v101, v57, v53, s41\n"
    "v_perm_b32 v98, v58, v54, s40\n"
    "v_perm_b32 v102, v58, v54, s41\n"
    "v_perm_b32 v99, v59, v55, s40\n"
    "v_perm_b32 v103, v59, v55, s41\n"
    "v_dot2c_f32_bf16 v88, v96, v77\n"
    "v_dot2c_f32_bf16 v89, v100, v77\n"
    "v_dot2c_f32_bf16 v90, v97, v77\n"
    "v_dot2c_f32_bf16 v91, v101, v77\n"
    "v_dot2c_f32_bf16 v92, v98, v77\n"
    "v_dot2c_f32_bf16 v93, v102, v77\n"
    "v_dot2c_f32_bf16 v94, v99, v77\n"
    "v_dot2c_f32_bf16 v95, v103, v77\n"
    "s_waitcnt vmcnt(2)\n"
    "v_perm_b32 v96, v64, v60, s40\n"
    "v_perm_b32 v100, v64, v60, s41\n"
    "v_perm_b32 v97, v65, v61, s40\n"
    "v_perm_b32 v101, v65, v61, s41\n"
    "v_perm_b32 v98, v66, v62, s40\n"
    "v_perm_b32 v102, v66, v62, s41\n"
    "v_perm_b32 v99, v67, v63, s40\n"
    "v_perm_b32 v103, v67, v63, s41\n"
    "v_dot2c_f32_bf16 v88, v96, v78\n"
    "v_dot2c_f32_bf16 v89, v100, v78\n"
    "v_dot2c_f32_bf16 v90, v97, v78\n"
    "v_dot2c_f32_bf16 v91, v101, v78\n"
    "v_dot2c_f32_bf16 v92, v98, v78\n"
    "v_dot2c_f32_bf16 v93, v102, v78\n"
    "v_dot2c_f32_bf16 v94, v99, v78\n"
    "v_dot2c_f32_bf16 v95, v103, v78\n"
    "s_waitcnt vmcnt(0)\n"
    "v_perm_b32 v96, v72, v68, s40\n"
    "v_perm_b32 v100, v72, v68, s41\n"
    "v_perm_b32 v97, v73, v69, s40\n"
    "v_perm_b32 v101, v73, v69, s41\n"
    "v_perm_b32 v98, v74, v70, s40\n"
    "v_perm_b32 v102, v74, v70, s41\n"
    "v_perm_b32 v99, v75, v71, s40\n"
    "v_perm_b32 v103, v75, v71, s41\n"
    "v_dot2c_f32_bf16 v88, v96, v79\n"
    "v_dot2c_f32_bf16 v89, v100, v79\n"
    "v_dot2c_f32_bf16 v90, v97, v79\n"
    "v_dot2c_f32_bf16 v91, v101, v79\n"
    "v_dot2c_f32_bf16 v92, v98, v79\n"
    "v_dot2c_f32_bf16 v93, v102, v79\n"
    "v_dot2c_f32_bf16 v94, v99, v79\n"
    "v_dot2c_f32_bf16 v95, v103, v79\n"
    "v_permlane32_swap_b32 v80, v88\n"
    "v_permlane32_swap_b32 v81, v89\n"
    "v_permlane32_swap_b32 v82, v90\n"
    "v_permlane32_swap_b32 v83, v91\n"
    "v_permlane32_swap_b32 v84, v92\n"
    "v_permlane32_swap_b32 v85, v93\n"
    "v_permlane32_swap_b32 v86, v94\n"
    "v_permlane32_swap_b32 v87, v95\n"
    "v_pk_add_f32 v[80:81], v[80:81], v[88:89]\n"
    "v_pk_add_f32 v[82:83], v[82:83], v[90:91]\n"
    "v_pk_add_f32 v[84:85], v[84:85], v[92:93]\n"
    "v_pk_add_f32 v[86:87], v[86:87], v[94:95]\n"
    "s_nop 0\n"
    "v_permlane16_swap_b32 v80, v84\n"
    "v_permlane16_swap_b32 v81, v85\n"
    "v_permlane16_swap_b32 v82, v86\n"
    "v_permlane16_swap_b32 v83, v87\n"
    "v_pk_add_f32 v[80:81], v[80:81], v[84:85]\n"
    "v_pk_add_f32 v[82:83], v[82:83], v[86:87]\n"
    "s_nop 0\n"
    "v_add_f32_dpp v80, v80, v80 row_ror:8 row_mask:0xf bank_mask:0xf bound_ctrl:1\n"
    "v_add_f32_dpp v81, v81, v81 row_ror:8 row_mask:0xf bank_mask:0xf bound_ctrl:1\n"
    "v_add_f32_dpp v82, v82, v82 row_ror:8 row_mask:0xf bank_mask:0xf bound_ctrl:1\n"
    "v_add_f32_dpp v83, v83, v83 row_ror:8 row_mask:0xf bank_mask:0xf bound_ctrl:1\n"
    "v_add_u32 v88, 0x800, v80\n"
    "v_add_u32 v89, 0x800, v81\n"
    "v_add_u32 v90, 0x800, v82\n"
    "v_add_u32 v91, 0x800, v83\n"
    "v_and_or_b32 v88, v88, v6, s56\n"
    "v_and_or_b32 v89, v89, v6, s57\n"
    "v_and_or_b32 v90, v90, v6, s58\n"
    "v_and_or_b32 v91, v91, v6, s59\n"
    ".LW10_fused_decode:\n"
    "ds_read_b32 v7, v5 offset:3016\n"
    "s_waitcnt lgkmcnt(0)\n"
    "v_cmp_gt_u32 vcc, 4, v7\n"
    "s_nop 0\n"
    "s_cbranch_vccz .LW10_fused_decode_d\n"
    "s_sleep 1\n"
    "s_branch .LW10_fused_decode\n"
    ".LW10_fused_decode_d:\n"
    "buffer_store_dwordx4 v[88:91], v11, s[36:39], 0 offen offset:0 sc1\n"
    "v_bfrev_b32 v96, 1\n"
    "ds_read_b32 v27, v5 offset:2644\n"
    ".LW11_fused_decode:\n"
    "ds_read_b32 v7, v5 offset:3016\n"
    "s_waitcnt lgkmcnt(0)\n"
    "v_cmp_gt_u32 vcc, 4, v7\n"
    "s_nop 0\n"
    "s_cbranch_vccnz .LW11_fused_decode\n"
    "v_readfirstlane_b32 s46, v27\n"
    "s_nop 0\n"
    "s_add_u32 s46, s46, 300\n"
    ".LT12_fused_decode:\n"
    "s_memrealtime s[48:49]\n"
    "s_waitcnt lgkmcnt(0)\n"
    "s_sub_u32 s43, s48, s46\n"
    "s_bitcmp1_b32 s43, 19\n"
    "s_cbranch_scc1 .LT12_fused_decode\n"
    "s_movk_i32 s14, 0x3800\n"
    "s_mov_b32 s15, s31\n"
    "s_movk_i32 s33, 3584\n"
    "buffer_load_dwordx4 v[108:111], v106, s[12:15], 0 offen offset:0 nt\n"
    "buffer_load_dwordx4 v[112:115], v106, s[12:15], 0 offen offset:896 nt\n"
    "buffer_load_dwordx4 v[116:119], v106, s[12:15], s33 offen offset:0 nt\n"
    "buffer_load_dwordx4 v[120:123], v106, s[12:15], s33 offen offset:896 nt\n"
    "s_addk_i32 s33, 3584\n"
    "buffer_load_dwordx4 v[124:127], v106, s[12:15], s33 offen offset:0 nt\n"
    "buffer_load_dwordx4 v[128:131], v106, s[12:15], s33 offen offset:896 nt\n"
    "s_addk_i32 s33, 3584\n"
    "buffer_load_dwordx4 v[132:135], v106, s[12:15], s33 offen offset:0 nt\n"
    "buffer_load_dwordx4 v[136:139], v106, s[12:15], s33 offen offset:896 nt\n"
    "s_movk_i32 s33, 3584\n"
    "buffer_load_dwordx4 v[140:143], v106, s[8:11], 0 offen offset:0 nt\n"
    "buffer_load_dwordx4 v[144:147], v106, s[8:11], 0 offen offset:896 nt\n"
    "buffer_load_dwordx4 v[148:151], v106, s[8:11], s33 offen offset:0 nt\n"
    "buffer_load_dwordx4 v[152:155], v106, s[8:11], s33 offen offset:896 nt\n"
    "buffer_store_dword v5, v96, s[36:39], 0 offen\n"
    "s_branch .LAT_fused_decode\n"
    ".LA0Ts_fused_decode:\n"
    "s_setpc_b64 s[48:49]\n"
    ".LA0T_fused_decode:\n"
    "s_lshl_b32 s42, s26, 5\n"
    "s_add_u32 s42, s42, s27\n"
    "s_mul_i32 s43, s42, 0x3800\n"
    "s_add_u32 s12, s18, s43\n"
    "s_addc_u32 s13, s19, 0\n"
    "s_and_b32 s13, s13, 0xffff\n"
    "s_mul_i32 s43, s42, 0x1c00\n"
    "s_add_u32 s8, s20, s43\n"
    "s_addc_u32 s9, s21, 0\n"
    "s_and_b32 s9, s9, 0xffff\n"
    "s_cmp_eq_u32 s26, 7\n"
    "s_cselect_b32 s10, 0, 0x1c00\n"
    "s_mov_b32 s11, s31\n"
    "s_lshl_b32 s42, s42, 6\n"
    "s_add_u32 s42, s42, 872448\n"
    "v_and_b32 v104, 31, v1\n"
    "v_lshrrev_b32 v105, 5, v1\n"
    "v_mul_u32_u24 v106, 1792, v105\n"
    "v_lshl_add_u32 v106, v104, 4, v106\n"
    "v_and_b32 v107, 15, v1\n"
    "v_lshlrev_b32 v107, 14, v107\n"
    "v_and_b32 v7, 48, v1\n"
    "v_add3_u32 v107, v107, v7, s42\n"
    "v_add_u32 v156, 0x40000, v107\n"
    "v_mov_b32 v97, 1\n"
    "v_lshl_add_u32 v90, v1, 4, v99\n"
    "v_add_u32 v90, 1024, v90\n"
    "v_mov_b32 v93, s57\n"
    "v_mov_b32 v94, s58\n"
    "v_mov_b32 v95, s59\n"
    "v_bfrev_b32 v96, 1\n"
    ".LW3_fused_decode:\n"
    "ds_read_b32 v7, v5 offset:3032\n"
    "s_waitcnt lgkmcnt(0)\n"
    "v_cmp_gt_u32 vcc, 4, v7\n"
    "s_nop 0\n"
    "s_cbranch_vccz .LW3_fused_decode_d\n"
    "s_sleep 1\n"
    "s_branch .LW3_fused_decode\n"
    ".LW3_fused_decode_d:\n"
    "s_mov_b64 exec, 1\n"
    "buffer_atomic_add v97, v99, s[36:39], 0 offen sc0\n"
    "s_mov_b64 exec, -1\n"
    "ds_read_b32 v88, v5 offset:2644\n"
    "s_waitcnt lgkmcnt(0)\n"
    "v_readfirstlane_b32 s44, v88\n"
    "s_mov_b32 s45, 0\n"
    "s_add_u32 s44, s44, 300\n"
    ".LLA_fused_decode:\n"
    "s_sleep 1\n"
    "s_getreg_b32 s43, hwreg(HW_REG_IB_STS, 0, 4)\n"
    "s_or_b32 s43, s43, s45\n"
    "s_cbranch_scc1 .LLB_fused_decode\n"
    "s_mov_b32 s45, 1\n"
    "s_waitcnt vmcnt(0)\n"
    "s_memrealtime s[46:47]\n"
    "v_readfirstlane_b32 s52, v97\n"
    "s_nop 0\n"
    "s_cmp_eq_u32 s52, 31\n"
    "s_cbranch_scc0 .LNL4_fused_decode\n"
    "s_waitcnt lgkmcnt(0)\n"
    "s_lshl_b32 s43, s46, 12\n"
    "v_mov_b32 v91, s43\n"
    "s_or_b32 s43, s43, s56\n"
    "v_mov_b32 v92, s43\n"
    "buffer_store_dwordx4 v[92:95], v98, s[36:39], 0 offen offset:0 sc1\n"
    "buffer_store_dwordx4 v[92:95], v98, s[36:39], 0 offen offset:1024 sc1\n"
    "s_mov_b64 exec, 15\n"
    "buffer_store_dwordx4 v[92:95], v90, s[36:39], 0 offen offset:0 sc1\n"
    "ds_write_b32 v5, v91 offset:2640\n"
    "s_mov_b64 exec, 1\n"
    "ds_add_u32 v5, v4 offset:3040\n"
    "s_mov_b64 exec, -1\n"
    ".LNL4_fused_decode:\n"
    ".LLB_fused_decode:\n"
    "ds_read_b32 v7, v5 offset:3016\n"
    "s_memrealtime s[48:49]\n"
    "s_waitcnt lgkmcnt(0)\n"
    "v_cmp_gt_u32 vcc, 4, v7\n"
    "s_sub_u32 s43, s48, s44\n"
    "s_cbranch_vccnz .LLA_fused_decode\n"
    "s_bitcmp1_b32 s43, 19\n"
    "s_cbranch_scc1 .LLA_fused_decode\n"
    "s_movk_i32 s14, 0x3800\n"
    "s_mov_b32 s15, s31\n"
    "s_movk_i32 s33, 3584\n"
    "buffer_load_dwordx4 v[108:111], v106, s[12:15], 0 offen offset:0 nt\n"
    "buffer_load_dwordx4 v[112:115], v106, s[12:15], 0 offen offset:896 nt\n"
    "buffer_load_dwordx4 v[116:119], v106, s[12:15], s33 offen offset:0 nt\n"
    "buffer_load_dwordx4 v[120:123], v106, s[12:15], s33 offen offset:896 nt\n"
    "s_addk_i32 s33, 3584\n"
    "buffer_load_dwordx4 v[124:127], v106, s[12:15], s33 offen offset:0 nt\n"
    "buffer_load_dwordx4 v[128:131], v106, s[12:15], s33 offen offset:896 nt\n"
    "s_addk_i32 s33, 3584\n"
    "buffer_load_dwordx4 v[132:135], v106, s[12:15], s33 offen offset:0 nt\n"
    "buffer_load_dwordx4 v[136:139], v106, s[12:15], s33 offen offset:896 nt\n"
    "s_movk_i32 s33, 3584\n"
    "buffer_load_dwordx4 v[140:143], v106, s[8:11], 0 offen offset:0 nt\n"
    "buffer_load_dwordx4 v[144:147], v106, s[8:11], 0 offen offset:896 nt\n"
    "buffer_load_dwordx4 v[148:151], v106, s[8:11], s33 offen offset:0 nt\n"
    "buffer_load_dwordx4 v[152:155], v106, s[8:11], s33 offen offset:896 nt\n"
    "s_cmp_eq_u32 s45, 0\n"
    "s_cbranch_scc0 .LLU_fused_decode\n"
    "s_waitcnt vmcnt(12)\n"
    "s_memrealtime s[46:47]\n"
    "v_readfirstlane_b32 s52, v97\n"
    "s_nop 0\n"
    "s_cmp_eq_u32 s52, 31\n"
    "s_cbranch_scc0 .LNL5_fused_decode\n"
    "s_waitcnt lgkmcnt(0)\n"
    "s_lshl_b32 s43, s46, 12\n"
    "v_mov_b32 v91, s43\n"
    "s_or_b32 s43, s43, s56\n"
    "v_mov_b32 v92, s43\n"
    "buffer_store_dwordx4 v[92:95], v98, s[36:39], 0 offen offset:0 sc1\n"
    "buffer_store_dwordx4 v[92:95], v98, s[36:39], 0 offen offset:1024 sc1\n"
    "s_mov_b64 exec, 15\n"
    "buffer_store_dwordx4 v[92:95], v90, s[36:39], 0 offen offset:0 sc1\n"
    "ds_write_b32 v5, v91 offset:2640\n"
    "s_mov_b64 exec, 1\n"
    "ds_add_u32 v5, v4 offset:3040\n"
    "s_mov_b64 exec, -1\n"
    "s_waitcnt vmcnt(3)\n"
    ".LNL5_fused_decode:\n"
    ".LLU_fused_decode:\n"
    "buffer_store_dword v5, v96, s[36:39], 0 offen\n"
    "s_branch .LAT_fused_decode\n"
    ".p2align 12\n"
    ".LKB_fused_decode:\n"
    "s_load_dwordx2 s[18:19], s[0:1], 0x38\n"
    "s_load_dwordx4 s[20:23], s[0:1], 0x40\n"
    "s_mov_b32 s36, s8\n"
    "s_and_b32 s37, s9, 0xffff\n"
    "v_and_b32 v9, 31, v1\n"
    "v_lshlrev_b32 v9, 4, v9\n"
    "v_mov_b32 v84, 0\n"
    "v_mov_b32 v85, 0\n"
    "v_mov_b32 v86, 0\n"
    "v_mov_b32 v87, 0\n"
    "v_mov_b32 v88, 0\n"
    "v_mov_b32 v89, 0\n"
    "v_mov_b32 v90, 0\n"
    "v_mov_b32 v91, 0\n"
    "v_mov_b32 v92, 0\n"
    "v_mov_b32 v93, 0\n"
    "v_mov_b32 v94, 0\n"
    "v_mov_b32 v95, 0\n"
    "v_mov_b32 v96, 0\n"
    "v_mov_b32 v97, 0\n"
    "v_mov_b32 v98, 0\n"
    "v_mov_b32 v99, 0\n"
    "v_mov_b32 v100, 0\n"
    "v_mov_b32 v101, 0\n"
    "v_mov_b32 v102, 0\n"
    "v_mov_b32 v103, 0\n"
    "v_mov_b32 v104, 0\n"
    "v_mov_b32 v105, 0\n"
    "v_mov_b32 v106, 0\n"
    "v_mov_b32 v107, 0\n"
    "v_mov_b32 v108, 0\n"
    "v_mov_b32 v109, 0\n"
    "v_mov_b32 v110, 0\n"
    "v_mov_b32 v111, 0\n"
    "v_mov_b32 v112, 0\n"
    "v_mov_b32 v113, 0\n"
    "v_mov_b32 v114, 0\n"
    "v_mov_b32 v115, 0\n"
    ".LW19_fused_decode:\n"
    "ds_read_b32 v7, v5 offset:3008\n"
    "s_waitcnt lgkmcnt(0)\n"
    "v_cmp_gt_u32 vcc, 1, v7\n"
    "s_nop 0\n"
    "s_cbranch_vccz .LW19_fused_decode_d\n"
    "s_sleep 1\n"
    "s_branch .LW19_fused_decode\n"
    ".LW19_fused_decode_d:\n"
    "ds_read_b128 v[76:79], v9 offset:1024\n"
    "ds_read_b128 v[80:83], v9 offset:1536\n"
    "s_waitcnt lgkmcnt(0)\n"
    "v_dot2c_f32_bf16 v84, v12, v76\n"
    "v_dot2c_f32_bf16 v100, v12, v80\n"
    "v_dot2c_f32_bf16 v85, v16, v76\n"
    "v_dot2c_f32_bf16 v101, v16, v80\n"
    "v_dot2c_f32_bf16 v84, v13, v77\n"
    "v_dot2c_f32_bf16 v100, v13, v81\n"
    "v_dot2c_f32_bf16 v85, v17, v77\n"
    "v_dot2c_f32_bf16 v101, v17, v81\n"
    "v_dot2c_f32_bf16 v84, v14, v78\n"
    "v_dot2c_f32_bf16 v100, v14, v82\n"
    "v_dot2c_f32_bf16 v85, v18, v78\n"
    "v_dot2c_f32_bf16 v101, v18, v82\n"
    "v_dot2c_f32_bf16 v84, v15, v79\n"
    "v_dot2c_f32_bf16 v100, v15, v83\n"
    "v_dot2c_f32_bf16 v85, v19, v79\n"
    "v_dot2c_f32_bf16 v101, v19, v83\n"
    "v_dot2c_f32_bf16 v86, v20, v76\n"
    "v_dot2c_f32_bf16 v102, v20, v80\n"
    "v_dot2c_f32_bf16 v87, v24, v76\n"
    "v_dot2c_f32_bf16 v103, v24, v80\n"
    "v_dot2c_f32_bf16 v86, v21, v77\n"
    "v_dot2c_f32_bf16 v102, v21, v81\n"
    "v_dot2c_f32_bf16 v87, v25, v77\n"
    "v_dot2c_f32_bf16 v103, v25, v81\n"
    "v_dot2c_f32_bf16 v86, v22, v78\n"
    "v_dot2c_f32_bf16 v102, v22, v82\n"
    "v_dot2c_f32_bf16 v87, v26, v78\n"
    "v_dot2c_f32_bf16 v103, v26, v82\n"
    "v_dot2c_f32_bf16 v86, v23, v79\n"
    "v_dot2c_f32_bf16 v102, v23, v83\n"
    "v_dot2c_f32_bf16 v87, v27, v79\n"
    "v_dot2c_f32_bf16 v103, v27, v83\n"
    "v_permlane16_swap_b32 v84, v100\n"
    "v_permlane16_swap_b32 v85, v101\n"
    "v_add_f32 v84, v84, v100\n"
    "v_add_f32 v85, v85, v101\n"
    "v_dot2c_f32_bf16 v88, v28, v76\n"
    "v_dot2c_f32_bf16 v104, v28, v80\n"
    "v_dot2c_f32_bf16 v89, v32, v76\n"
    "v_dot2c_f32_bf16 v105, v32, v80\n"
    "v_dot2c_f32_bf16 v88, v29, v77\n"
    "v_dot2c_f32_bf16 v104, v29, v81\n"
    "v_dot2c_f32_bf16 v89, v33, v77\n"
    "v_dot2c_f32_bf16 v105, v33, v81\n"
    "v_dot2c_f32_bf16 v88, v30, v78\n"
    "v_dot2c_f32_bf16 v104, v30, v82\n"
    "v_dot2c_f32_bf16 v89, v34, v78\n"
    "v_dot2c_f32_bf16 v105, v34, v82\n"
    "v_dot2c_f32_bf16 v88, v31, v79\n"
    "v_dot2c_f32_bf16 v104, v31, v83\n"
    "v_dot2c_f32_bf16 v89, v35, v79\n"
    "v_dot2c_f32_bf16 v105, v35, v83\n"
    "v_permlane16_swap_b32 v86, v102\n"
    "v_permlane16_swap_b32 v87, v103\n"
    "v_add_f32 v86, v86, v102\n"
    "v_add_f32 v87, v87, v103\n"
    "v_dot2c_f32_bf16 v90, v36, v76\n"
    "v_dot2c_f32_bf16 v106, v36, v80\n"
    "v_dot2c_f32_bf16 v91, v40, v76\n"
    "v_dot2c_f32_bf16 v107, v40, v80\n"
    "v_dot2c_f32_bf16 v90, v37, v77\n"
    "v_dot2c_f32_bf16 v106, v37, v81\n"
    "v_dot2c_f32_bf16 v91, v41, v77\n"
    "v_dot2c_f32_bf16 v107, v41, v81\n"
    "v_dot2c_f32_bf16 v90, v38, v78\n"
    "v_dot2c_f32_bf16 v106, v38, v82\n"
    "v_dot2c_f32_bf16 v91, v42, v78\n"
    "v_dot2c_f32_bf16 v107, v42, v82\n"
    "v_dot2c_f32_bf16 v90, v39, v79\n"
    "v_dot2c_f32_bf16 v106, v39, v83\n"
    "v_dot2c_f32_bf16 v91, v43, v79\n"
    "v_dot2c_f32_bf16 v107, v43, v83\n"
    "v_permlane16_swap_b32 v88, v104\n"
    "v_permlane16_swap_b32 v89, v105\n"
    "v_add_f32 v88, v88, v104\n"
    "v_add_f32 v89, v89, v105\n"
    "v_dot2c_f32_bf16 v92, v44, v76\n"
    "v_dot2c_f32_bf16 v108, v44, v80\n"
    "v_dot2c_f32_bf16 v93, v48, v76\n"
    "v_dot2c_f32_bf16 v109, v48, v80\n"
    "v_dot2c_f32_bf16 v92, v45, v77\n"
    "v_dot2c_f32_bf16 v108, v45, v81\n"
    "v_dot2c_f32_bf16 v93, v49, v77\n"
    "v_dot2c_f32_bf16 v109, v49, v81\n"
    "v_dot2c_f32_bf16 v92, v46, v78\n"
    "v_dot2c_f32_bf16 v108, v46, v82\n"
    "v_dot2c_f32_bf16 v93, v50, v78\n"
    "v_dot2c_f32_bf16 v109, v50, v82\n"
    "v_dot2c_f32_bf16 v92, v47, v79\n"
    "v_dot2c_f32_bf16 v108, v47, v83\n"
    "v_dot2c_f32_bf16 v93, v51, v79\n"
    "v_dot2c_f32_bf16 v109, v51, v83\n"
    "v_permlane16_swap_b32 v90, v106\n"
    "v_permlane16_swap_b32 v91, v107\n"
    "v_add_f32 v90, v90, v106\n"
    "v_add_f32 v91, v91, v107\n"
    "s_waitcnt vmcnt(4)\n"
    "v_dot2c_f32_bf16 v94, v52, v76\n"
    "v_dot2c_f32_bf16 v110, v52, v80\n"
    "v_dot2c_f32_bf16 v95, v56, v76\n"
    "v_dot2c_f32_bf16 v111, v56, v80\n"
    "v_dot2c_f32_bf16 v94, v53, v77\n"
    "v_dot2c_f32_bf16 v110, v53, v81\n"
    "v_dot2c_f32_bf16 v95, v57, v77\n"
    "v_dot2c_f32_bf16 v111, v57, v81\n"
    "v_dot2c_f32_bf16 v94, v54, v78\n"
    "v_dot2c_f32_bf16 v110, v54, v82\n"
    "v_dot2c_f32_bf16 v95, v58, v78\n"
    "v_dot2c_f32_bf16 v111, v58, v82\n"
    "v_dot2c_f32_bf16 v94, v55, v79\n"
    "v_dot2c_f32_bf16 v110, v55, v83\n"
    "v_dot2c_f32_bf16 v95, v59, v79\n"
    "v_dot2c_f32_bf16 v111, v59, v83\n"
    "v_permlane16_swap_b32 v92, v108\n"
    "v_permlane16_swap_b32 v93, v109\n"
    "v_add_f32 v92, v92, v108\n"
    "v_add_f32 v93, v93, v109\n"
    "s_waitcnt vmcnt(2)\n"
    "v_dot2c_f32_bf16 v96, v60, v76\n"
    "v_dot2c_f32_bf16 v112, v60, v80\n"
    "v_dot2c_f32_bf16 v97, v64, v76\n"
    "v_dot2c_f32_bf16 v113, v64, v80\n"
    "v_dot2c_f32_bf16 v96, v61, v77\n"
    "v_dot2c_f32_bf16 v112, v61, v81\n"
    "v_dot2c_f32_bf16 v97, v65, v77\n"
    "v_dot2c_f32_bf16 v113, v65, v81\n"
    "v_dot2c_f32_bf16 v96, v62, v78\n"
    "v_dot2c_f32_bf16 v112, v62, v82\n"
    "v_dot2c_f32_bf16 v97, v66, v78\n"
    "v_dot2c_f32_bf16 v113, v66, v82\n"
    "v_dot2c_f32_bf16 v96, v63, v79\n"
    "v_dot2c_f32_bf16 v112, v63, v83\n"
    "v_dot2c_f32_bf16 v97, v67, v79\n"
    "v_dot2c_f32_bf16 v113, v67, v83\n"
    "v_permlane16_swap_b32 v94, v110\n"
    "v_permlane16_swap_b32 v95, v111\n"
    "v_add_f32 v94, v94, v110\n"
    "v_add_f32 v95, v95, v111\n"
    "s_waitcnt vmcnt(0)\n"
    "v_dot2c_f32_bf16 v98, v68, v76\n"
    "v_dot2c_f32_bf16 v114, v68, v80\n"
    "v_dot2c_f32_bf16 v99, v72, v76\n"
    "v_dot2c_f32_bf16 v115, v72, v80\n"
    "v_dot2c_f32_bf16 v98, v69, v77\n"
    "v_dot2c_f32_bf16 v114, v69, v81\n"
    "v_dot2c_f32_bf16 v99, v73, v77\n"
    "v_dot2c_f32_bf16 v115, v73, v81\n"
    "v_dot2c_f32_bf16 v98, v70, v78\n"
    "v_dot2c_f32_bf16 v114, v70, v82\n"
    "v_dot2c_f32_bf16 v99, v74, v78\n"
    "v_dot2c_f32_bf16 v115, v74, v82\n"
    "v_dot2c_f32_bf16 v98, v71, v79\n"
    "v_dot2c_f32_bf16 v114, v71, v83\n"
    "v_dot2c_f32_bf16 v99, v75, v79\n"
    "v_dot2c_f32_bf16 v115, v75, v83\n"
    "v_permlane16_swap_b32 v96, v112\n"
    "v_permlane16_swap_b32 v97, v113\n"
    "v_add_f32 v96, v96, v112\n"
    "v_add_f32 v97, v97, v113\n"
    "v_permlane16_swap_b32 v98, v114\n"
    "v_permlane16_swap_b32 v99, v115\n"
    "v_add_f32 v98, v98, v114\n"
    "v_add_f32 v99, v99, v115\n"
    "v_add_f32_dpp v100, v84, v84 row_ror:8 row_mask:0xf bank_mask:0x3\n"
    "v_add_f32_dpp v101, v85, v85 row_ror:8 row_mask:0xf bank_mask:0x3\n"
    "v_add_f32_dpp v102, v86, v86 row_ror:8 row_mask:0xf bank_mask:0x3\n"
    "v_add_f32_dpp v103, v87, v87 row_ror:8 row_mask:0xf bank_mask:0x3\n"
    "v_add_f32_dpp v104, v88, v88 row_ror:8 row_mask:0xf bank_mask:0x3\n"
    "v_add_f32_dpp v105, v89, v89 row_ror:8 row_mask:0xf bank_mask:0x3\n"
    "v_add_f32_dpp v106, v90, v90 row_ror:8 row_mask:0xf bank_mask:0x3\n"
    "v_add_f32_dpp v107, v91, v91 row_ror:8 row_mask:0xf bank_mask:0x3\n"
    "v_add_f32_dpp v100, v92, v92 row_ror:8 row_mask:0xf bank_mask:0xc\n"
    "v_add_f32_dpp v101, v93, v93 row_ror:8 row_mask:0xf bank_mask:0xc\n"
    "v_add_f32_dpp v102, v94, v94 row_ror:8 row_mask:0xf bank_mask:0xc\n"
    "v_add_f32_dpp v103, v95, v95 row_ror:8 row_mask:0xf bank_mask:0xc\n"
    "v_add_f32_dpp v104, v96, v96 row_ror:8 row_mask:0xf bank_mask:0xc\n"
    "v_add_f32_dpp v105, v97, v97 row_ror:8 row_mask:0xf bank_mask:0xc\n"
    "v_add_f32_dpp v106, v98, v98 row_ror:8 row_mask:0xf bank_mask:0xc\n"
    "v_add_f32_dpp v107, v99, v99 row_ror:8 row_mask:0xf bank_mask:0xc\n"
    "v_add_f32_dpp v84, v100, v100 row_half_mirror row_mask:0xf bank_mask:0x5\n"
    "v_add_f32_dpp v85, v101, v101 row_half_mirror row_mask:0xf bank_mask:0x5\n"
    "v_add_f32_dpp v86, v102, v102 row_half_mirror row_mask:0xf bank_mask:0x5\n"
    "v_add_f32_dpp v87, v103, v103 row_half_mirror row_mask:0xf bank_mask:0x5\n"
    "v_add_f32_dpp v84, v104, v104 row_half_mirror row_mask:0xf bank_mask:0xa\n"
    "v_add_f32_dpp v85, v105, v105 row_half_mirror row_mask:0xf bank_mask:0xa\n"
    "v_add_f32_dpp v86, v106, v106 row_half_mirror row_mask:0xf bank_mask:0xa\n"
    "v_add_f32_dpp v87, v107, v107 row_half_mirror row_mask:0xf bank_mask:0xa\n"
    "v_and_b32 v10, 2, v1\n"
    "v_cmp_ne_u32 vcc, 0, v10\n"
    "v_add_f32_dpp v84, v84, v84 quad_perm:[2,3,0,1] row_mask:0xf bank_mask:0xf bound_ctrl:1\n"
    "v_add_f32_dpp v85, v85, v85 quad_perm:[2,3,0,1] row_mask:0xf bank_mask:0xf bound_ctrl:1\n"
    "v_add_f32_dpp v86, v86, v86 quad_perm:[2,3,0,1] row_mask:0xf bank_mask:0xf bound_ctrl:1\n"
    "v_add_f32_dpp v87, v87, v87 quad_perm:[2,3,0,1] row_mask:0xf bank_mask:0xf bound_ctrl:1\n"
    "v_cndmask_b32 v84, v84, v86, vcc\n"
    "v_cndmask_b32 v85, v85, v87, vcc\n"
    "v_and_b32 v10, 1, v1\n"
    "v_cmp_ne_u32 vcc, 0, v10\n"
    "v_add_f32_dpp v84, v84, v84 quad_perm:[1,0,3,2] row_mask:0xf bank_mask:0xf bound_ctrl:1\n"
    "v_add_f32_dpp v85, v85, v85 quad_perm:[1,0,3,2] row_mask:0xf bank_mask:0xf bound_ctrl:1\n"
    "v_cndmask_b32 v84, v84, v85, vcc\n"
    "v_cvt_pk_bf16_f32 v84, v84, v84\n"
    "v_lshlrev_b32 v84, 16, v84\n"
    "v_mul_f32 v84, 0x3d800000, v84\n"
    "s_nop 1\n"
    "v_max_f32_dpp v85, v84, v84 quad_perm:[1,0,3,2] row_mask:0xf bank_mask:0xf bound_ctrl:1\n"
    "s_nop 1\n"
    "v_max_f32_dpp v85, v85, v85 quad_perm:[2,3,0,1] row_mask:0xf bank_mask:0xf bound_ctrl:1\n"
    "s_nop 1\n"
    "v_max_f32_dpp v85, v85, v85 row_half_mirror row_mask:0xf bank_mask:0xf bound_ctrl:1\n"
    "s_nop 1\n"
    "v_max_f32_dpp v85, v85, v85 row_ror:8 row_mask:0xf bank_mask:0xf bound_ctrl:1\n"
    "v_mov_b32 v86, v85\n"
    "s_nop 1\n"
    "v_permlane32_swap_b32 v85, v86\n"
    "v_max_f32 v85, v85, v86\n"
    "v_bfe_u32 v10, v1, 4, 1\n"
    "v_lshlrev_b32 v10, 2, v10\n"
    "s_lshl_b32 s42, s25, 3\n"
    "v_add_u32 v11, s42, v10\n"
    "ds_write_b32 v11, v85 offset:2560\n"
    "s_mov_b64 exec, 1\n"
    "ds_add_u32 v5, v4 offset:3020\n"
    "s_mov_b64 exec, -1\n"
    "s_lshl_b32 s42, s25, 7\n"
    "v_lshrrev_b32 v9, 5, v1\n"
    "v_lshlrev_b32 v9, 6, v9\n"
    "v_lshl_add_u32 v9, v10, 3, v9\n"
    "v_and_b32 v86, 14, v1\n"
    "v_lshl_add_u32 v9, v86, 1, v9\n"
    "v_add_u32 v9, s42, v9\n"
    "v_add_u32 v90, 2560, v10\n"
    ".LW20_fused_decode:\n"
    "ds_read_b32 v7, v5 offset:3020\n"
    "s_waitcnt lgkmcnt(0)\n"
    "v_cmp_gt_u32 vcc, 4, v7\n"
    "s_nop 0\n"
    "s_cbranch_vccnz .LW20_fused_decode\n"
    "ds_read2_b32 v[86:87], v90 offset0:0 offset1:2\n"
    "ds_read2_b32 v[88:89], v90 offset0:4 offset1:6\n"
    "s_waitcnt lgkmcnt(0)\n"
    "v_max3_f32 v85, v86, v87, v88\n"
    "v_max_f32 v85, v85, v89\n"
    "v_sub_f32 v84, v84, v85\n"
    "v_mul_f32 v84, 0x3fb8aa3b, v84\n"
    "v_exp_f32 v84, v84\n"
    "s_nop 1\n"
    "v_mov_b32_dpp v86, v84 quad_perm:[1,0,3,2] row_mask:0xf bank_mask:0xf bound_ctrl:1\n"
    "v_add_f32 v87, v84, v86\n"
    "v_cvt_pk_bf16_f32 v86, v84, v86\n"
    "s_mov_b64 exec, 0x55555555\n"
    "s_mov_b32 exec_hi, 0x55555555\n"
    "ds_write_b32 v9, v86 offset:2048\n"
    "s_mov_b64 exec, -1\n"
    "v_add_f32_dpp v87, v87, v87 quad_perm:[2,3,0,1] row_mask:0xf bank_mask:0xf bound_ctrl:1\n"
    "s_nop 1\n"
    "v_add_f32_dpp v87, v87, v87 row_half_mirror row_mask:0xf bank_mask:0xf bound_ctrl:1\n"
    "s_nop 1\n"
    "v_add_f32_dpp v87, v87, v87 row_ror:8 row_mask:0xf bank_mask:0xf bound_ctrl:1\n"
    "v_mov_b32 v88, v87\n"
    "s_nop 1\n"
    "v_permlane32_swap_b32 v87, v88\n"
    "v_add_f32 v87, v87, v88\n"
    "ds_write_b32 v11, v87 offset:2592\n"
    "s_mov_b64 exec, 1\n"
    "ds_add_u32 v5, v4 offset:3024\n"
    "s_mov_b64 exec, -1\n"
    "s_setprio 0\n"
    "ds_read_b128 v[36:39], v5 offset:2624\n"
    "s_waitcnt lgkmcnt(0)\n"
    "v_readfirstlane_b32 s56, v36\n"
    "v_readfirstlane_b32 s57, v37\n"
    "v_readfirstlane_b32 s58, v38\n"
    "v_readfirstlane_b32 s59, v39\n"
    "v_lshlrev_b32 v26, 7, v3\n"
    "v_add_u32 v26, 2048, v26\n"
    "v_lshlrev_b32 v30, 1, v3\n"
    "s_lshl_b32 s42, s16, 4\n"
    "v_add_u32 v30, s42, v30\n"
    "s_cmp_lt_u32 s16, 56\n"
    "s_cbranch_scc0 .LKC_fused_decode\n"
    "ds_read_u16 v31, v30 offset:3072\n"
    ".LKC_fused_decode:\n"
    "s_cmp_eq_u32 s25, 0\n"
    "s_cbranch_scc0 .LK1_fused_decode\n"
    "s_lshl_b32 s42, s26, 11\n"
    "s_add_u32 s42, s42, 4096\n"
    "v_mov_b32 v25, s42\n"
    "v_and_b32 v8, 31, v1\n"
    "v_mul_u32_u24 v8, 3328, v8\n"
    "s_mul_i32 s43, s27, 80\n"
    "s_add_u32 s43, s43, s54\n"
    "s_add_u32 s43, s43, 768\n"
    "v_add_u32 v8, s43, v8\n"
    "v_and_b32 v9, 32, v1\n"
    "v_add_u32 v9, v8, v9\n"
    "v_and_b32 v10, 8, v1\n"
    "v_lshlrev_b32 v10, 1, v10\n"
    "v_lshrrev_b32 v11, 4, v1\n"
    "v_lshl_add_u32 v10, v11, 2, v10\n"
    "s_bitcmp1_b32 s27, 0\n"
    "s_cselect_b32 s44, 64, 0\n"
    "s_add_u32 s44, s44, s42\n"
    "s_add_u32 s45, s42, 2048\n"
    ".LW21_fused_decode:\n"
    "ds_read_b32 v7, v5 offset:3028\n"
    "s_waitcnt lgkmcnt(0)\n"
    "v_cmp_gt_u32 vcc, 4, v7\n"
    "s_nop 0\n"
    "s_cbranch_vccnz .LW21_fused_decode\n"
    "s_setprio 2\n"
    ".LSP_fused_decode:\n"
    "ds_read_b32 v7, v5 offset:3040\n"
    "ds_read_b32 v72, v5 offset:2640\n"
    "s_load_dwordx4 s[48:51], s[8:9], s44\n"
    "s_waitcnt lgkmcnt(0)\n"
    "v_cmp_ne_u32 vcc, 0, v7\n"
    "s_nop 0\n"
    "s_cbranch_vccnz .LGK_fused_decode\n"
    "s_xor_b64 s[48:49], s[48:49], s[56:57]\n"
    "s_xor_b64 s[50:51], s[50:51], s[58:59]\n"
    "s_or_b32 s43, s48, s49\n"
    "s_or_b32 s43, s43, s50\n"
    "s_or_b32 s43, s43, s51\n"
    "v_mov_b32 v72, s48\n"
    "s_and_b32 s43, s43, 0xfff\n"
    "s_cbranch_scc0 .LGS_fused_decode\n"
    "s_add_u32 s44, s44, 128\n"
    "s_sleep 0\n"
    "s_cmp_lt_u32 s44, s45\n"
    "s_cbranch_scc1 .LSP_fused_decode\n"
    "s_mov_b32 s34, 131072\n"
    ".LG22_fused_decode:\n"
    "buffer_load_dwordx4 v[72:75], v25, s[36:39], 0 offen offset:0 sc1\n"
    "s_waitcnt vmcnt(0)\n"
    "v_xor_b32 v72, s56, v72\n"
    "v_xor_b32 v73, s57, v73\n"
    "v_xor_b32 v74, s58, v74\n"
    "v_xor_b32 v75, s59, v75\n"
    "v_or3_b32 v7, v72, v73, v74\n"
    "v_or_b32 v7, v7, v75\n"
    "v_and_b32 v7, 0xfff, v7\n"
    "v_cmp_ne_u32 vcc, 0, v7\n"
    "s_nop 0\n"
    "s_cbranch_vccz .LG22_fused_decode_d\n"
    "s_mov_b64 exec, vcc\n"
    "s_sleep 4\n"
    "s_sub_u32 s34, s34, 1\n"
    "s_cbranch_scc0 .LG22_fused_decode\n"
    ".LG22_fused_decode_d:\n"
    "s_mov_b64 exec, -1\n"
    ".LGS_fused_decode:\n"
    "ds_write_b32 v5, v72 offset:2640\n"
    "s_mov_b64 exec, 1\n"
    "ds_add_u32 v5, v4 offset:3040\n"
    "s_mov_b64 exec, -1\n"
    ".LGK_fused_decode:\n"
    "v_readfirstlane_b32 s46, v72\n"
    "s_nop 0\n"
    "s_lshr_b32 s46, s46, 12\n"
    "s_add_u32 s46, s46, 66\n"
    ".LT23_fused_decode:\n"
    "s_memrealtime s[48:49]\n"
    "s_waitcnt lgkmcnt(0)\n"
    "s_sub_u32 s43, s48, s46\n"
    "s_bitcmp1_b32 s43, 19\n"
    "s_cbranch_scc1 .LT23_fused_decode\n"
    "s_setprio 3\n"
    "s_mov_b32 s34, 131072\n"
    ".LG24_fused_decode:\n"
    "buffer_load_dwordx4 v[12:15], v8, s[36:39], 0 offen offset:64 sc1\n"
    "buffer_load_dwordx4 v[16:19], v9, s[36:39], 0 offen offset:0 sc1\n"
    "buffer_load_dwordx4 v[20:23], v9, s[36:39], 0 offen offset:16 sc1\n"
    "s_waitcnt vmcnt(2)\n"
    "v_xor_b32 v12, s56, v12\n"
    "v_xor_b32 v13, s57, v13\n"
    "v_xor_b32 v14, s58, v14\n"
    "v_xor_b32 v15, s59, v15\n"
    "v_or3_b32 v7, v12, v13, v14\n"
    "v_or_b32 v7, v7, v15\n"
    "s_waitcnt vmcnt(1)\n"
    "v_xor_b32 v16, s56, v16\n"
    "v_xor_b32 v17, s57, v17\n"
    "v_xor_b32 v18, s58, v18\n"
    "v_xor_b32 v19, s59, v19\n"
    "v_or3_b32 v7, v7, v16, v17\n"
    "v_or3_b32 v7, v7, v18, v19\n"
    "s_waitcnt vmcnt(0)\n"
    "v_xor_b32 v20, s56, v20\n"
    "v_xor_b32 v21, s57, v21\n"
    "v_xor_b32 v22, s58, v22\n"
    "v_xor_b32 v23, s59, v23\n"
    "v_or3_b32 v7, v7, v20, v21\n"
    "v_or3_b32 v7, v7, v22, v23\n"
    "v_and_b32 v7, 0xfff, v7\n"
    "v_cmp_ne_u32 vcc, 0, v7\n"
    "s_nop 0\n"
    "s_cbranch_vccz .LG24_fused_decode_d\n"
    "s_mov_b64 exec, vcc\n"
    "s_sleep 1\n"
    "s_sub_u32 s34, s34, 1\n"
    "s_cbranch_scc0 .LG24_fused_decode\n"
    ".LG24_fused_decode_d:\n"
    "s_mov_b64 exec, -1\n"
    "v_max_f32_dpp v14, v12, v12 quad_perm:[1,0,3,2] row_mask:0xf bank_mask:0xf bound_ctrl:1\n"
    "s_nop 1\n"
    "v_max_f32_dpp v14, v14, v14 quad_perm:[2,3,0,1] row_mask:0xf bank_mask:0xf bound_ctrl:1\n"
    "s_nop 1\n"
    "v_max_f32_dpp v14, v14, v14 row_half_mirror row_mask:0xf bank_mask:0xf bound_ctrl:1\n"
    "s_nop 1\n"
    "v_max_f32_dpp v14, v14, v14 row_ror:8 row_mask:0xf bank_mask:0xf bound_ctrl:1\n"
    "v_mov_b32 v15, v14\n"
    "s_nop 1\n"
    "v_permlane16_swap_b32 v14, v15\n"
    "v_max_f32 v14, v14, v15\n"
    "v_sub_f32 v12, v12, v14\n"
    "v_mul_f32 v12, 0x3fb8aa3b, v12\n"
    "v_exp_f32 v12, v12\n"
    "s_nop 0\n"
    "v_mul_f32 v13, v13, v12\n"
    "v_mov_b32 v15, v13\n"
    "v_pk_mul_f32 v[16:17], v[16:17], v[12:13] op_sel_hi:[1,0]\n"
    "v_pk_mul_f32 v[18:19], v[18:19], v[12:13] op_sel_hi:[1,0]\n"
    "v_pk_mul_f32 v[20:21], v[20:21], v[12:13] op_sel_hi:[1,0]\n"
    "v_pk_mul_f32 v[22:23], v[22:23], v[12:13] op_sel_hi:[1,0]\n"
    "v_permlane16_swap_b32 v13, v15\n"
    "v_permlane16_swap_b32 v16, v20\n"
    "v_permlane16_swap_b32 v17, v21\n"
    "v_permlane16_swap_b32 v18, v22\n"
    "v_permlane16_swap_b32 v19, v23\n"
    "v_add_f32 v13, v13, v15\n"
    "v_pk_add_f32 v[16:17], v[16:17], v[20:21]\n"
    "v_pk_add_f32 v[18:19], v[18:19], v[22:23]\n"
    "v_add_f32_dpp v13, v13, v13 row_ror:8 row_mask:0xf bank_mask:0xf bound_ctrl:1\n"
    "v_add_f32_dpp v20, v16, v16 row_ror:8 row_mask:0xf bank_mask:0x3\n"
    "v_add_f32_dpp v21, v17, v17 row_ror:8 row_mask:0xf bank_mask:0x3\n"
    "s_nop 0\n"
    "v_add_f32_dpp v20, v18, v18 row_ror:8 row_mask:0xf bank_mask:0xc\n"
    "v_add_f32_dpp v21, v19, v19 row_ror:8 row_mask:0xf bank_mask:0xc\n"
    "v_add_f32_dpp v13, v13, v13 quad_perm:[1,0,3,2] row_mask:0xf bank_mask:0xf bound_ctrl:1\n"
    "v_add_f32_dpp v20, v20, v20 quad_perm:[1,0,3,2] row_mask:0xf bank_mask:0xf bound_ctrl:1\n"
    "v_add_f32_dpp v21, v21, v21 quad_perm:[1,0,3,2] row_mask:0xf bank_mask:0xf bound_ctrl:1\n"
    "v_add_f32_dpp v13, v13, v13 quad_perm:[2,3,0,1] row_mask:0xf bank_mask:0xf bound_ctrl:1\n"
    "v_add_f32_dpp v20, v20, v20 quad_perm:[2,3,0,1] row_mask:0xf bank_mask:0xf bound_ctrl:1\n"
    "v_add_f32_dpp v21, v21, v21 quad_perm:[2,3,0,1] row_mask:0xf bank_mask:0xf bound_ctrl:1\n"
    "v_add_f32_dpp v13, v13, v13 row_half_mirror row_mask:0xf bank_mask:0xf bound_ctrl:1\n"
    "v_add_f32_dpp v20, v20, v20 row_half_mirror row_mask:0xf bank_mask:0xf bound_ctrl:1\n"
    "v_add_f32_dpp v21, v21, v21 row_half_mirror row_mask:0xf bank_mask:0xf bound_ctrl:1\n"
    "v_rcp_f32 v13, v13\n"
    "s_nop 0\n"
    "v_mul_f32 v20, v20, v13\n"
    "v_mul_f32 v21, v21, v13\n"
    "v_cvt_pk_bf16_f32 v20, v20, v21\n"
    "ds_write_b32 v10, v20 offset:2656\n"
    "s_mov_b64 exec, 1\n"
    "ds_add_u32 v5, v4 offset:3044\n"
    "s_mov_b64 exec, -1\n"
    "s_branch .LKE_fused_decode\n"
    ".LK1_fused_decode:\n"
    "s_cmp_eq_u32 s25, 1\n"
    "s_cbranch_scc0 .LKE_fused_decode\n"
    "s_cmp_eq_u32 s26, 7\n"
    "s_cbranch_scc1 .LKE_fused_decode\n"
    "s_and_b32 s43, s27, 0x18\n"
    "s_mul_i32 s43, s43, 3328\n"
    "s_add_u32 s43, s43, s54\n"
    "s_and_b32 s42, s27, 7\n"
    "s_lshl_b32 s42, s42, 5\n"
    "s_add_u32 s43, s43, s42\n"
    "s_add_u32 s43, s43, 256\n"
    "v_and_b32 v10, 16, v1\n"
    "v_lshlrev_b32 v10, 4, v10\n"
    "v_and_b32 v11, 8, v1\n"
    "v_lshl_add_u32 v10, v11, 1, v10\n"
    "v_mul_u32_u24 v9, 3328, v3\n"
    "v_add3_u32 v10, v10, v9, s43\n"
    "v_lshrrev_b32 v11, 1, v11\n"
    "v_and_b32 v9, 2, v1\n"
    "v_cmp_ne_u32 s[44:45], 0, v9\n"
    "v_lshl_add_u32 v11, v9, 2, v11\n"
    "v_and_b32 v9, 1, v1\n"
    "v_cmp_ne_u32 s[50:51], 0, v9\n"
    "v_mov_b32 v24, 0xbdd2d3e8\n"
    "v_mov_b32 v25, 0xc0135761\n"
    ".LW25_fused_decode:\n"
    "ds_read_b32 v7, v5 offset:3040\n"
    "ds_read_b32 v12, v5 offset:2640\n"
    "s_waitcnt lgkmcnt(0)\n"
    "v_cmp_eq_u32 vcc, 0, v7\n"
    "s_nop 0\n"
    "s_cbranch_vccnz .LW25_fused_decode\n"
    "v_readfirstlane_b32 s46, v12\n"
    "s_nop 0\n"
    "s_lshr_b32 s46, s46, 12\n"
    "s_add_u32 s46, s46, 40\n"
    ".LT26_fused_decode:\n"
    "s_memrealtime s[48:49]\n"
    "s_waitcnt lgkmcnt(0)\n"
    "s_sub_u32 s43, s48, s46\n"
    "s_bitcmp1_b32 s43, 19\n"
    "s_cbranch_scc1 .LT26_fused_decode\n"
    "s_setprio 2\n"
    "s_mov_b32 s34, 131072\n"
    ".LG27_fused_decode:\n"
    "buffer_load_dwordx4 v[12:15], v10, s[36:39], 0 offen offset:0 sc1\n"
    "s_waitcnt vmcnt(0)\n"
    "v_xor_b32 v12, s56, v12\n"
    "v_xor_b32 v13, s57, v13\n"
    "v_xor_b32 v14, s58, v14\n"
    "v_xor_b32 v15, s59, v15\n"
    "v_or3_b32 v7, v12, v13, v14\n"
    "v_or_b32 v7, v7, v15\n"
    "v_and_b32 v7, 0xfff, v7\n"
    "v_cmp_ne_u32 vcc, 0, v7\n"
    "s_nop 0\n"
    "s_cbranch_vccz .LG27_fused_decode_d\n"
    "s_mov_b64 exec, vcc\n"
    "s_sleep 1\n"
    "s_sub_u32 s34, s34, 1\n"
    "s_cbranch_scc0 .LG27_fused_decode\n"
    ".LG27_fused_decode_d:\n"
    "s_mov_b64 exec, -1\n"
    "v_add_f32_dpp v12, v12, v12 row_half_mirror row_mask:0xf bank_mask:0xf bound_ctrl:1\n"
    "v_add_f32_dpp v13, v13, v13 row_half_mirror row_mask:0xf bank_mask:0xf bound_ctrl:1\n"
    "v_add_f32_dpp v14, v14, v14 row_half_mirror row_mask:0xf bank_mask:0xf bound_ctrl:1\n"
    "v_add_f32_dpp v15, v15, v15 row_half_mirror row_mask:0xf bank_mask:0xf bound_ctrl:1\n"
    "v_add_f32_dpp v12, v12, v12 quad_perm:[2,3,0,1] row_mask:0xf bank_mask:0xf bound_ctrl:1\n"
    "v_add_f32_dpp v13, v13, v13 quad_perm:[2,3,0,1] row_mask:0xf bank_mask:0xf bound_ctrl:1\n"
    "v_add_f32_dpp v14, v14, v14 quad_perm:[2,3,0,1] row_mask:0xf bank_mask:0xf bound_ctrl:1\n"
    "v_add_f32_dpp v15, v15, v15 quad_perm:[2,3,0,1] row_mask:0xf bank_mask:0xf bound_ctrl:1\n"
    "v_cndmask_b32 v12, v12, v14, s[44:45]\n"
    "v_cndmask_b32 v13, v13, v15, s[44:45]\n"
    "s_nop 0\n"
    "v_add_f32_dpp v12, v12, v12 quad_perm:[1,0,3,2] row_mask:0xf bank_mask:0xf bound_ctrl:1\n"
    "v_add_f32_dpp v13, v13, v13 quad_perm:[1,0,3,2] row_mask:0xf bank_mask:0xf bound_ctrl:1\n"
    "v_cndmask_b32 v12, v12, v13, s[50:51]\n"
    "v_mov_b32 v13, v12\n"
    "s_nop 1\n"
    "v_permlane16_swap_b32 v12, v13\n"
    "v_cvt_pk_bf16_f32 v12, v12, v13\n"
    "v_and_b32 v16, 0xffff0000, v12\n"
    "v_lshlrev_b32 v12, 16, v12\n"
    "v_mul_f32 v20, v12, v12\n"
    "v_fma_f32 v20, v20, v24, v25\n"
    "v_mul_f32 v20, v20, v12\n"
    "v_exp_f32 v20, v20\n"
    "s_nop 0\n"
    "v_add_f32 v20, 1.0, v20\n"
    "v_rcp_f32 v20, v20\n"
    "s_nop 0\n"
    "v_mul_f32 v12, v12, v20\n"
    "v_cvt_pk_bf16_f32 v12, v12, v12\n"
    "v_lshlrev_b32 v12, 16, v12\n"
    "v_mul_f32 v12, v12, v16\n"
    "s_nop 1\n"
    "v_mov_b32_dpp v13, v12 quad_perm:[1,0,3,2] row_mask:0xf bank_mask:0xf bound_ctrl:1\n"
    "v_cvt_pk_bf16_f32 v12, v12, v13\n"
    "s_mov_b64 exec, 0x505\n"
    "ds_write_b32 v11, v12 offset:2688\n"
    "s_mov_b64 exec, -1\n"
    "s_mov_b64 exec, 1\n"
    "ds_add_u32 v5, v4 offset:3048\n"
    "s_mov_b64 exec, -1\n"
    ".LKE_fused_decode:\n"
    "s_cmp_lt_u32 s16, 56\n"
    "s_cbranch_scc1 .LFIN_fused_decode\n"
    "s_endpgm\n"
    :: "{s[0:1]}"(__builtin_amdgcn_kernarg_segment_ptr()), "{s2}"(blockIdx.x), "{v0}"(threadIdx.x), "s"((unsigned)(size_t)(__attribute__((address_space(3))) char*)smem)
    : "v1", "v2", "v3", "v4", "v5", "v6", "v7", "v8", "v9", "v10", "v11", "v12", "v13", "v14", "v15", "v16", "v17", "v18", "v19", "v20", "v21", "v22", "v23", "v24", "v25", "v26", "v27", "v28", "v29", "v30", "v31", "v32", "v33", "v34", "v35", "v36", "v37", "v38", "v39", "v40", "v41", "v42", "v43", "v44", "v45", "v46", "v47", "v48", "v49", "v50", "v51", "v52", "v53", "v54", "v55", "v56", "v57", "v58", "v59", "v60", "v61", "v62", "v63", "v64", "v65", "v66", "v67", "v68", "v69", "v70", "v71", "v72", "v73", "v74", "v75", "v76", "v77", "v78", "v79", "v80", "v81", "v82", "v83", "v84", "v85", "v86", "v87", "v88", "v89", "v90", "v91", "v92", "v93", "v94", "v95", "v96", "v97", "v98", "v99", "v100", "v101", "v102", "v103", "v104", "v105", "v106", "v107", "v108", "v109", "v110", "v111", "v112", "v113", "v114", "v115", "v116", "v117", "v118", "v119", "v120", "v121", "v122", "v123", "v124", "v125", "v126", "v127", "v128", "v129", "v130", "v131", "v132", "v133", "v134", "v135", "v136", "v137", "v138", "v139", "v140", "v141", "v142", "v143", "v144", "v145", "v146", "v147", "v148", "v149", "v150", "v151", "v152", "v153", "v154", "v155", "v156", "s3", "s4", "s5", "s6", "s7", "s8", "s9", "s10", "s11", "s12", "s13", "s14", "s15", "s16", "s17", "s18", "s19", "s20", "s21", "s22", "s23", "s24", "s25", "s26", "s27", "s28", "s29", "s30", "s31", "s33", "s34", "s35", "s36", "s37", "s38", "s39", "s40", "s41", "s42", "s43", "s44", "s45", "s46", "s47", "s48", "s49", "s50", "s51", "s52", "s53", "s54", "s55", "s56", "s57", "s58", "s59", "vcc", "memory");
}

extern "C" void kernel_launch(void* const* d_in, const int* in_sizes, int n_in, void* d_out, int out_size, void* d_ws, size_t ws_size, hipStream_t stream) {
  fused_decode<<<256, 640, 0, stream>>>(d_in[1], d_in[0], d_in[3], d_ws, d_in[2], d_in[6], d_in[7], d_in[4], d_in[8], d_out);
}
```
